```python
import torch

def _softmax(x, axis=-1):
    return torch.softmax(x, dim=axis)
def _gelu(x):
    return torch.nn.functional.gelu(x, approximate="tanh")
def _silu(x):
    return torch.nn.functional.silu(x)
def _sigmoid(x):
    return torch.sigmoid(x)
def _layernorm(x, axis=-1, eps=1e-5):
    m = x.mean(dim=axis, keepdim=True)
    v = x.var(dim=axis, keepdim=True, unbiased=False)
    return (x - m) / torch.sqrt(v + eps)
def _scatter_add(tgt, idx, src, axis=0):
    return tgt.clone().index_add_(axis, idx, src)
def _segment_sum(src, seg, n):
    out = torch.zeros((n,) + src.shape[1:], dtype=src.dtype)
    return out.index_add_(0, seg, src)
def _pad(x, pw):
    flat = tuple(v for pair in reversed(pw) for v in pair)
    return torch.nn.functional.pad(x, flat)

kernel_name = 'gated_mix__ssm_conv_s4__glu_gated_swiglu__prompt'

def setup_inputs():

    _w = (lambda a: (lambda t: t.to(torch.bfloat16) if t.is_floating_point() else t)(torch.as_tensor(a)))
    def _A_setup():
        B, L, D, N = 1, 2048, 512, 128
        A_re = -(lambda _x: torch.exp(torch.as_tensor(_x)))((lambda *s: torch.randn(*s, dtype=torch.bfloat16) * 3.2)(D, N))
        A_im = torch.pi * (lambda *s: torch.randn(*s, dtype=torch.bfloat16) * 0.05)(D, N)
        B_re = (lambda *s: torch.randn(*s, dtype=torch.bfloat16) * 0.1)(D, N)
        B_im = (lambda *s: torch.randn(*s, dtype=torch.bfloat16) * 0.1)(D, N)
        C_re = (lambda *s: torch.randn(*s, dtype=torch.bfloat16) * 0.1)(D, N)
        C_im = (lambda *s: torch.randn(*s, dtype=torch.bfloat16) * 0.1)(D, N)
        log_dt = (lambda *s: torch.randn(*s, dtype=torch.bfloat16) * 0.05)(D) * 0.5 - 4.0
        Dskip = (lambda *s: torch.randn(*s, dtype=torch.bfloat16) * 0.05)(D)
        return {
            "u": _w((lambda *s: torch.randn(*s, dtype=torch.bfloat16) * 0.05)(B, L, D)),
            "A_re": _w(A_re),
            "A_im": _w(A_im),
            "B_re": _w(B_re),
            "B_im": _w(B_im),
            "C_re": _w(C_re),
            "C_im": _w(C_im),
            "log_dt": _w(log_dt),
            "Dskip": _w(Dskip),
        }
    _A_in = _A_setup()

    def _B_setup():
        B, S, D, F = 1, 2048, 512, 1792
        return {
            "x": _w((lambda *s: torch.randn(*s, dtype=torch.bfloat16) * 0.05)(B, S, D)),
            "w1": _w((lambda *s: torch.randn(*s, dtype=torch.bfloat16) * 0.8)(D, F) * (2.0 / D) ** 0.5),
            "w3": _w((lambda *s: torch.randn(*s, dtype=torch.bfloat16) * 0.8)(D, F) * (2.0 / D) ** 0.5),
            "w2": _w((lambda *s: torch.randn(*s, dtype=torch.bfloat16) * 0.8)(F, D) * (2.0 / F) ** 0.5),
        }
    _B_in = _B_setup()

    return {
        "A_u": _A_in["u"],
        "A_A_re": _A_in["A_re"],
        "A_A_im": _A_in["A_im"],
        "A_B_re": _A_in["B_re"],
        "A_B_im": _A_in["B_im"],
        "A_C_re": _A_in["C_re"],
        "A_C_im": _A_in["C_im"],
        "A_log_dt": _A_in["log_dt"],
        "A_Dskip": _A_in["Dskip"],
        "B_x": _B_in["x"],
        "B_w1": _B_in["w1"],
        "B_w3": _B_in["w3"],
        "B_w2": _B_in["w2"],
        "gate_w": _w((lambda *s: torch.randn(*s, dtype=torch.bfloat16) * 0.05)(512) * 16.0),
    }

def reference(A_u, A_A_re, A_A_im, A_B_re, A_B_im, A_C_re, A_C_im, A_log_dt, A_Dskip, B_x, B_w1, B_w3, B_w2, gate_w):
    def _A_ref(u, A_re, A_im, B_re, B_im, C_re, C_im, log_dt, Dskip):
        Bsz, L, D = u.shape
        dt = torch.exp(log_dt)[:, None]
        A = A_re + 1j * A_im
        Bc = B_re + 1j * B_im
        Cc = C_re + 1j * C_im
        logdA = dt * A
        dB = dt * Bc
        l = (lambda x, dt: x.to(dt))(torch.arange(L), A_re.dtype)
        P = torch.exp(logdA[:, :, None] * l[None, None, :])
        K = torch.einsum("dn,dnl,dn->dl", Cc, P, dB).real
        L2 = 2 * L
        Kp = _pad(K, ((0, 0), (0, L)))
        up = (lambda x, *d: x.permute(*d))(u, 0, 2, 1)
        up = _pad(up, ((0, 0), (0, 0), (0, L)))
        Kf = (lambda x, axes=(-2,-1): torch.fft.rfft2(x.float(), dim=axes))(Kp[:, None, :], axes=(-2, -1))
        Uf = (lambda x, axes=(-2,-1): torch.fft.rfft2(x.float(), dim=axes))(up[:, :, None, :], axes=(-2, -1))
        Yf = Uf * Kf[None, :, :, :]
        y = (lambda x, s, axes=(-2,-1): torch.fft.irfft2(x, s=s, dim=axes).to(torch.bfloat16))(Yf, (1, L2), axes=(-2, -1))[:, :, 0, :L]
        y = (lambda x, *d: x.permute(*d))(y, 0, 2, 1) + u * Dskip[None, None, :]
        return y

    def _B_ref(x, w1, w3, w2):
        gate = torch.einsum("bsd,df->bsf", x, w1)
        up = torch.einsum("bsd,df->bsf", x, w3)
        h = _silu(gate) * up
        out = torch.einsum("bsf,fd->bsd", h, w2)
        return out

    x = A_u
    g = _sigmoid((x * gate_w).sum(-1, keepdims=True))
    a = _A_ref(x, A_A_re, A_A_im, A_B_re, A_B_im, A_C_re, A_C_im, A_log_dt, A_Dskip)
    b = _B_ref(x, B_w1, B_w3, B_w2)
    return g * a + (1 - g) * b
```

```cpp
#include <hip/hip_runtime.h>
#include <stdint.h>

typedef unsigned short u16;
typedef __bf16 bf16x8 __attribute__((ext_vector_type(8)));
typedef __bf16 bf16x2 __attribute__((ext_vector_type(2)));
typedef short s16x4 __attribute__((ext_vector_type(4)));
typedef float f32x2 __attribute__((ext_vector_type(2)));
typedef float f32x4 __attribute__((ext_vector_type(4)));
typedef float f32x16 __attribute__((ext_vector_type(16)));
typedef unsigned u32x2 __attribute__((ext_vector_type(2)));
typedef unsigned u32x4 __attribute__((ext_vector_type(4)));
#define LAS __attribute__((address_space(3)))
#define GAS __attribute__((address_space(1)))
#define DI __device__ __forceinline__

constexpr int SL = 2048, SD = 512, SN = 128, SF = 1792;
constexpr size_t OFF_HT = 0;
constexpr size_t OFF_W2T = OFF_HT + (size_t)SL * SF * 2;
constexpr size_t OFF_UT = OFF_W2T + (size_t)SF * SD * 2;
constexpr size_t OFF_KR = OFF_UT + (size_t)SL * SD * 2;
constexpr size_t OFF_AT = OFF_KR + (size_t)SL * SD * 2;
constexpr size_t OFF_G = OFF_AT + (size_t)SL * SD * 2;
constexpr size_t OFF_CNT = OFF_G + (size_t)SL * 4;

DI float bf2f(unsigned v) { return __builtin_bit_cast(float, v << 16); }
DI float bfhi(unsigned v) { return __builtin_bit_cast(float, v & 0xffff0000u); }
DI unsigned pk2(float a, float b) { f32x2 v = {a, b}; return __builtin_bit_cast(unsigned, __builtin_convertvector(v, bf16x2)); }
DI float rbf(float a) { return bf2f(pk2(a, 0.f) & 0xffffu); }
DI void lgkm0() { asm volatile("s_waitcnt lgkmcnt(0)" ::: "memory"); }
DI void bar() { asm volatile("s_waitcnt lgkmcnt(0)\n\ts_barrier" ::: "memory"); }
DI void glds16(const void* sbase, unsigned voff, unsigned lds) {
  asm volatile("s_mov_b32 m0, %0\n\ts_nop 0\n\tglobal_load_lds_dwordx4 %1, %2" ::"s"(lds), "v"(voff), "s"(sbase) : "memory");
}
DI u32x4 lds128(unsigned a) { return *(const LAS u32x4*)(uintptr_t)a; }
DI void sts128(unsigned a, u32x4 v) { *(LAS u32x4*)(uintptr_t)a = v; }
DI bf16x8 trfrag(unsigned a0, unsigned a1) {
  s16x4 lo = __builtin_amdgcn_ds_read_tr16_b64_v4i16((LAS s16x4*)(uintptr_t)a0);
  s16x4 hi = __builtin_amdgcn_ds_read_tr16_b64_v4i16((LAS s16x4*)(uintptr_t)a1);
  u32x2 l = __builtin_bit_cast(u32x2, lo), h = __builtin_bit_cast(u32x2, hi);
  u32x4 r = {l.x, l.y, h.x, h.y};
  return __builtin_bit_cast(bf16x8, r);
}
DI f32x16 mfma32(bf16x8 a, bf16x8 b, f32x16 c) { return __builtin_amdgcn_mfma_f32_32x32x16_bf16(a, b, c, 0, 0, 0); }
DI f32x4 mfma16(bf16x8 a, bf16x8 b, f32x4 c) { return __builtin_amdgcn_mfma_f32_16x16x32_bf16(a, b, c, 0, 0, 0); }

struct P1 {
  const u16 *x, *are, *aim, *bre, *bim, *cre, *cim, *ldt, *w1, *w3, *w2, *gw;
  char* ws;
};

constexpr unsigned STG = 49152, L1_PAR = 3 * STG, L1_KT = L1_PAR + 4096, L1_TOTAL = L1_KT + 12288;

DI void kgen_params(const P1& p, int ch0, int t, unsigned lb) {
  const int ch = ch0 + (t >> 7), o = ch * SN + (t & 127);
  const float ar = bf2f(p.are[o]), ai = bf2f(p.aim[o]), br = bf2f(p.bre[o]), bi = bf2f(p.bim[o]);
  const float cr = bf2f(p.cre[o]), ci = bf2f(p.cim[o]);
  const float dt = rbf(expf(bf2f(p.ldt[ch])));
  const float dbr = dt * br, dbi = dt * bi;
  float rev = (dt * ai) * 0.15915494309189535f;
  rev -= rintf(rev);
  f32x4 v = {(dt * ar) * 1.4426950408889634f, rev, cr * dbr - ci * dbi, cr * dbi + ci * dbr};
  sts128(lb + L1_PAR + t * 16, __builtin_bit_cast(u32x4, v));
}
template <bool FR> DI void cexp(float a, float rev, float x, float& re, float& im) {
  const float m = __builtin_amdgcn_exp2f(a * x);
  float ph = rev * x;
  if (FR) ph = __builtin_amdgcn_fractf(ph);
  re = m * __builtin_amdgcn_cosf(ph);
  im = m * __builtin_amdgcn_sinf(ph);
}
struct KG { f32x4 acc[3]; float xa0, xa1, xa2, xb; };
DI void kgen_init(KG& k, int lane) {
  const int r = lane & 15;
  k.xa0 = (float)r; k.xb = (float)(16 * r);
  k.xa1 = (float)(r < 8 ? 256 + 2 * r : 512 + 256 * ((r - 8) >> 2) + 4 * (r & 3));
  k.xa2 = (float)(r < 8 ? 1024 + 256 * (r >> 1) + 8 * (r & 1) : (r == 8 ? 2048 : 0));
  for (int i = 0; i < 3; ++i) k.acc[i] = f32x4{0.f, 0.f, 0.f, 0.f};
}
DI void kgen_step(KG& k, int hw, int ks, int lane, unsigned lb) {
  u32x4 a0, a1, a2, b;
  const unsigned pa = lb + L1_PAR + ((hw >> 1) * 128 + (hw & 1) * 64 + ks * 16 + (lane >> 4) * 4) * 16;
#pragma unroll
  for (int i = 0; i < 4; ++i) {
    const f32x4 q = __builtin_bit_cast(f32x4, lds128(pa + i * 16));
    float re, im;
    cexp<false>(q.x, q.y, k.xb, re, im);
    b[i] = pk2(q.z * re - q.w * im, -(q.z * im + q.w * re));
    cexp<false>(q.x, q.y, k.xa0, re, im); a0[i] = pk2(re, im);
    cexp<true>(q.x, q.y, k.xa1, re, im); a1[i] = pk2(re, im);
    cexp<true>(q.x, q.y, k.xa2, re, im); a2[i] = pk2(re, im);
  }
  const bf16x8 bb = __builtin_bit_cast(bf16x8, b);
  k.acc[0] = mfma16(__builtin_bit_cast(bf16x8, a0), bb, k.acc[0]);
  k.acc[1] = mfma16(__builtin_bit_cast(bf16x8, a1), bb, k.acc[1]);
  k.acc[2] = mfma16(__builtin_bit_cast(bf16x8, a2), bb, k.acc[2]);
}
DI void kgen_put(const KG& k, int hw, int lane, unsigned lb) {
  LAS float* kt = (LAS float*)(uintptr_t)(lb + L1_KT + hw * 3072);
#pragma unroll
  for (int t = 0; t < 3; ++t)
#pragma unroll
    for (int e = 0; e < 4; ++e) kt[(16 * t + 4 * (lane >> 4) + e) * 16 + (lane & 15)] = k.acc[t][e];
}
DI void kgen_expand(const P1& p, int ch0, int hw, int lane, unsigned lb) {
  const LAS float* kt = (const LAS float*)(uintptr_t)(lb + L1_KT + (hw >> 1) * 6144);
  u16* kr = (u16*)(p.ws + OFF_KR) + (size_t)(ch0 + (hw >> 1)) * SL;
#pragma unroll
  for (int rd = 0; rd < 2; ++rd) {
    const int i = rd * 128 + (hw & 1) * 64 + lane;
    float v[8];
#pragma unroll
    for (int e = 0; e < 8; ++e) {
      const int lq = (int)rbf((float)(2047 - 8 * i - e));
      const int hi = lq >> 8, lo = lq & 15, j = (lq >> 4) & 15;
      const int row = hi == 0 ? lo : hi == 1 ? 16 + (lo >> 1) : hi < 4 ? 16 + hi * 4 + (lo >> 2) : 24 + hi * 2 + (lo >> 3);
      v[e] = kt[row * 16 + j] + kt[768 + row * 16 + j];
    }
    u32x4 o = {pk2(v[0], v[1]), pk2(v[2], v[3]), pk2(v[4], v[5]), pk2(v[6], v[7])};
    *(u32x4*)(kr + 8 * i) = o;
  }
}

__global__ __launch_bounds__(512, 2) void k1(P1 p) {
  extern __shared__ __attribute__((aligned(16))) char smem[];
  const unsigned lb = (unsigned)(uintptr_t)(LAS char*)smem;
  const int b = blockIdx.x, xcd = b & 7, jj = b >> 3, tid = threadIdx.x, lane = tid & 63;
  const int wv = __builtin_amdgcn_readfirstlane(tid >> 6);
  const int ch0 = 2 * b;
  if (jj < 28) {
    const int mt = (xcd >> 1) * 4 + (jj & 3), ft = (xcd & 1) * 7 + (jj >> 2);
    if (wv >= 4) {
      const int hw = wv - 4;
      const unsigned vx0 = (lane >> 3) * 1024 + (((lane & 7) ^ ((lane >> 4) & 7)) << 4);
      const unsigned vx1 = (lane >> 3) * 1024 + (((lane & 7) ^ ((4 + (lane >> 4)) & 7)) << 4);
      const unsigned vw1 = (lane >> 4) * (SF * 2) + (((lane & 15) ^ ((lane >> 4) << 2)) << 4);
      const unsigned vw3 = (lane >> 4) * (SF * 2) + (((lane & 15) ^ (((lane >> 4) << 2) | 2)) << 4);
      const char* xb = (const char*)p.x + (size_t)(mt * 128 + hw * 32) * 1024;
      const char* w1b = (const char*)p.w1 + (size_t)(hw * 16) * (SF * 2) + ft * 256;
      const char* w3b = (const char*)p.w3 + (size_t)(hw * 16) * (SF * 2) + ft * 256;
      auto issue = [&](int st) {
        const unsigned d = lb + (st % 3) * STG + hw * 4096;
#pragma unroll
        for (int q = 0; q < 4; ++q) {
          glds16(w1b + (size_t)(st * 64 + q * 4) * (SF * 2), vw1, d + 16384 + q * 1024);
          glds16(w3b + (size_t)(st * 64 + q * 4) * (SF * 2), vw3, d + 32768 + q * 1024);
          glds16(xb + q * 8192 + st * 128, (q & 1) ? vx1 : vx0, d + q * 1024);
        }
      };
      issue(0); issue(1);
      KG k; kgen_init(k, lane);
#pragma unroll
      for (int i = 0; i < 8; ++i) {
        if (i < 7) asm volatile("s_waitcnt vmcnt(12)" ::: "memory");
        else asm volatile("s_waitcnt vmcnt(0)" ::: "memory");
        bar();
        if (i < 6) issue(i + 2);
        if (i < 4) kgen_step(k, hw, i, lane, lb);
        if (i == 4) kgen_put(k, hw, lane, lb);
        if (i == 7) kgen_expand(p, ch0, hw, lane, lb);
      }
    } else {
      kgen_params(p, ch0, tid, lb);
      if (b == 0 && tid < 8) *(unsigned*)(p.ws + OFF_CNT + tid * 128) = 0u;
      const int wm = wv & 1, wn = wv >> 1, r = lane & 31, h = lane >> 5;
      const int g = lane >> 4, q = (lane & 15) >> 2, pp = lane & 3;
      unsigned xa[4], wa[4];
#pragma unroll
      for (int s = 0; s < 4; ++s) xa[s] = lb + (wm * 64 + r) * 128 + (((2 * s + h) ^ ((r >> 1) & 7)) << 4);
#pragma unroll
      for (int c = 0; c < 4; ++c)
        wa[c] = lb + 16384 + (g & 1) * 16384 + (8 * (g >> 1) + q) * 256 + (((4 * wn + c) ^ ((q << 1) | (g & 1))) << 5) +
                ((((pp & 1) << 1) | (pp >> 1)) << 3);
      f32x16 acc[2][4];
#pragma unroll
      for (int t = 0; t < 2; ++t)
#pragma unroll
        for (int c = 0; c < 4; ++c)
#pragma unroll
          for (int e = 0; e < 16; ++e) acc[t][c][e] = 0.f;
#pragma unroll
      for (int i = 0; i < 8; ++i) {
        bar();
        const unsigned so = (i % 3) * STG;
#pragma unroll
        for (int s = 0; s < 4; ++s) {
          const bf16x8 x0 = __builtin_bit_cast(bf16x8, lds128(xa[s] + so));
          const bf16x8 x1 = __builtin_bit_cast(bf16x8, lds128(xa[s] + so + 4096));
#pragma unroll
          for (int c = 0; c < 4; ++c) {
            const bf16x8 wf = trfrag(wa[c] + so + s * 4096, wa[c] + so + s * 4096 + 1024);
            acc[0][c] = mfma32(wf, x0, acc[0][c]);
            acc[1][c] = mfma32(wf, x1, acc[1][c]);
          }
        }
      }
#pragma unroll
      for (int t = 0; t < 2; ++t)
#pragma unroll
        for (int c = 0; c < 4; ++c) {
          float hv[8];
#pragma unroll
          for (int e = 0; e < 8; ++e) {
            const float gt = rbf(acc[t][c][e]), up = rbf(acc[t][c][8 + e]);
            const float sg = rbf(gt * __builtin_amdgcn_rcpf(1.f + __builtin_amdgcn_exp2f(gt * -1.4426950408889634f)));
            hv[e] = sg * up;
          }
          u32x4 o = {pk2(hv[0], hv[1]), pk2(hv[2], hv[3]), pk2(hv[4], hv[5]), pk2(hv[6], hv[7])};
          *(u32x4*)(p.ws + OFF_HT + ((size_t)((mt * 4 + wm * 2 + t) * 112 + ft * 8 + wn * 4 + c) << 10) + lane * 16) = o;
        }
    }
  } else {
    const int sp = xcd * 4 + (jj - 28);
    constexpr unsigned US = 1024 + 64, WS = 512 + 64, L_W = 64 * US;
    if (wv >= 4) {
      const int hw = wv - 4;
      KG k; kgen_init(k, lane);
      bar();
#pragma unroll
      for (int i = 0; i < 4; ++i) kgen_step(k, hw, i, lane, lb);
      kgen_put(k, hw, lane, lb);
      bar();
      kgen_expand(p, ch0, hw, lane, lb);
    } else {
      u32x4 uv[16], wq[14];
#pragma unroll
      for (int i = 0; i < 16; ++i) uv[i] = *(const u32x4*)(p.x + (size_t)(sp * 64 + wv * 16 + i) * SD + lane * 8);
#pragma unroll
      for (int i = 0; i < 14; ++i) {
        const int un = sp * 7 + (i >> 1), row = (i & 1) * 8 + (tid >> 5);
        wq[i] = *(const u32x4*)(p.w2 + (size_t)((un >> 1) * 16 + row) * SD + (un & 1) * 256 + (tid & 31) * 8);
      }
      const u32x4 gw = *(const u32x4*)(p.gw + lane * 8);
      kgen_params(p, ch0, tid, lb);
      bar();
      float gsel = 0.f;
#pragma unroll
      for (int i = 0; i < 16; ++i) {
        sts128(lb + (wv * 16 + i) * US + lane * 16, uv[i]);
        float s = 0.f;
#pragma unroll
        for (int e = 0; e < 4; ++e) s += rbf(bf2f(uv[i][e]) * bf2f(gw[e])) + rbf(bfhi(uv[i][e]) * bfhi(gw[e]));
#pragma unroll
        for (int o = 32; o > 0; o >>= 1) s += __shfl_xor(s, o);
        if (lane == i) gsel = s;
      }
      if (lane < 16) ((float*)(p.ws + OFF_G))[sp * 64 + wv * 16 + lane] = rbf(1.f / (1.f + expf(-rbf(gsel))));
#pragma unroll
      for (int i = 0; i < 14; ++i) sts128(lb + L_W + ((i >> 1) * 16 + (i & 1) * 8 + (tid >> 5)) * WS + (tid & 31) * 16, wq[i]);
      bar();
      const int g = lane >> 4, q = (lane & 15) >> 2, pp = lane & 3;
#pragma unroll
      for (int i = 0; i < 16; ++i) {
        const int cb = wv * 8 + (i >> 1), l0 = (i & 1) * 32 + g * 8;
        const unsigned a = lb + (l0 + q) * US + (cb * 16 + pp * 4) * 2;
        const bf16x8 f = trfrag(a, a + 4 * US);
        *(bf16x8*)((u16*)(p.ws + OFF_UT) + (size_t)(cb * 16 + (lane & 15)) * SL + sp * 64 + l0) = f;
      }
#pragma unroll
      for (int i = 0; i < 14; ++i) {
        const int un = sp * 7 + (i >> 1), db = (i & 1) * 4 + wv;
        const unsigned a = lb + L_W + ((i >> 1) * 16 + 8 * (g >> 1) + q) * WS + (db * 32 + (g & 1) * 16 + pp * 4) * 2;
        const bf16x8 f = trfrag(a, a + 4 * WS);
        *(bf16x8*)(p.ws + OFF_W2T + ((size_t)(((un & 1) * 8 + db) * 112 + (un >> 1)) << 10) + lane * 16) = f;
      }
    }
  }
}

struct P2 { const u16* dskip; u16* out; char* ws; };
constexpr unsigned KCS = 4160, L2_UI = 2 * 8 * KCS, UIS = 7680, L2_PX = L2_UI + 2 * UIS, L2_AT = L2_PX + 16384, ATS = 144;
constexpr unsigned L2_FL = L2_AT + 64 * ATS, L2_TOTAL = L2_FL + 64, RS = 272;
constexpr int PF = 9;

DI void pair_sync(unsigned a, unsigned target, int lane) {
  lgkm0();
  LAS unsigned* f = (LAS unsigned*)(uintptr_t)a;
  if (lane == 0) __hip_atomic_fetch_add(f, 1u, __ATOMIC_RELAXED, __HIP_MEMORY_SCOPE_WORKGROUP);
  while (__hip_atomic_load(f, __ATOMIC_RELAXED, __HIP_MEMORY_SCOPE_WORKGROUP) < target) __builtin_amdgcn_s_sleep(1);
  asm volatile("" ::: "memory");
}

__global__ __launch_bounds__(512, 2) void k2(P2 p) {
  extern __shared__ __attribute__((aligned(16))) char smem[];
  const unsigned lb = (unsigned)(uintptr_t)(LAS char*)smem;
  const int b = blockIdx.x, xcd = b & 7, jj = b >> 3, tid = threadIdx.x, lane = tid & 63;
  const int wv = __builtin_amdgcn_readfirstlane(tid >> 6);
  const int mt = (xcd >> 1) * 8 + (jj & 7), nt = (xcd & 1) * 4 + (jj >> 3);
  const int r = lane & 31, h = lane >> 5;
  const int ftok = 16 * (wv & 3) + (lane & 15), fo = 4 * (wv >> 2) + (lane >> 4);
  if (tid < 16) *(LAS unsigned*)(uintptr_t)(lb + L2_FL + tid * 4) = 0u;
  const float gate = ((const float*)(p.ws + OFF_G))[mt * 64 + ftok];
  if (wv < 4) {
    const char* wa = p.ws + OFF_W2T + ((size_t)(2 * nt * 112 + 28 * wv) << 10) + lane * 16;
    const char* hb = p.ws + OFF_HT + ((size_t)(2 * mt * 112 + 28 * wv) << 10) + lane * 16;
    bf16x8 A[PF][2], B[PF][2];
#pragma unroll
    for (int s = 0; s < PF; ++s) {
      B[s][0] = *(const bf16x8*)(hb + s * 1024); A[s][0] = *(const bf16x8*)(wa + s * 1024);
      B[s][1] = *(const bf16x8*)(hb + (112 + s) * 1024); A[s][1] = *(const bf16x8*)(wa + (112 + s) * 1024);
    }
    __builtin_amdgcn_sched_barrier(0);
    bar();
    f32x16 acc[2][2];
#pragma unroll
    for (int a = 0; a < 2; ++a)
#pragma unroll
      for (int t = 0; t < 2; ++t)
#pragma unroll
        for (int e = 0; e < 16; ++e) acc[a][t][e] = 0.f;
#pragma unroll
    for (int s = 0; s < 28; ++s) {
      const int u = s % PF;
      acc[0][0] = mfma32(A[u][0], B[u][0], acc[0][0]);
      acc[0][1] = mfma32(A[u][0], B[u][1], acc[0][1]);
      acc[1][0] = mfma32(A[u][1], B[u][0], acc[1][0]);
      acc[1][1] = mfma32(A[u][1], B[u][1], acc[1][1]);
      if (s + PF < 28) {
        const int n = s + PF;
        B[u][0] = *(const bf16x8*)(hb + n * 1024); A[u][0] = *(const bf16x8*)(wa + n * 1024);
        B[u][1] = *(const bf16x8*)(hb + (112 + n) * 1024); A[u][1] = *(const bf16x8*)(wa + (112 + n) * 1024);
      }
      __builtin_amdgcn_sched_barrier(0);
    }
    bar();
#pragma unroll
    for (int a = 0; a < 2; ++a)
#pragma unroll
      for (int t = 0; t < 2; ++t)
#pragma unroll
        for (int q = 0; q < 4; ++q) {
          f32x4 v = {acc[a][t][4 * q], acc[a][t][4 * q + 1], acc[a][t][4 * q + 2], acc[a][t][4 * q + 3]};
          sts128(lb + (wv * 64 + 32 * t + r) * RS + (32 * a + 8 * q + 4 * h) * 4, __builtin_bit_cast(u32x4, v));
        }
  } else {
    const int cw = wv - 4, cl = cw >> 1, half = cw & 1, ch = 2 * b + cl;
    const u16* kr = (const u16*)(p.ws + OFF_KR) + (size_t)ch * SL;
    const u16* ut = (const u16*)(p.ws + OFF_UT) + (size_t)ch * SL;
    const unsigned kc = lb + cl * 8 * KCS, ui = lb + L2_UI + cl * UIS;
    u32x4 k0[2], k1v[2], uu[2];
#pragma unroll
    for (int rd = 0; rd < 2; ++rd) {
      const int i = rd * 128 + half * 64 + lane;
      k0[rd] = *(const u32x4*)(kr + 8 * i);
      k1v[rd] = *(const u32x4*)(kr + 8 * (i < 255 ? i + 1 : i));
      uu[rd] = *(const u32x4*)(ut + 8 * i);
    }
    const float dsk = bf2f(p.dskip[ch]);
    bar();
    const u32x4 z4 = {0u, 0u, 0u, 0u};
    { const int i = half * 64 + lane; if (i < 124) sts128(ui + (i >> 2) * 80 + (i & 3) * 16, z4); }
    if (half == 0 && lane < 32) sts128(kc + (lane >> 2) * KCS + (256 + (lane & 3)) * 16, z4);
#pragma unroll
    for (int rd = 0; rd < 2; ++rd) {
      const int i = rd * 128 + half * 64 + lane;
      sts128(ui + (31 + (i >> 2)) * 80 + (i & 3) * 16, uu[rd]);
      unsigned w[8], o[7];
#pragma unroll
      for (int e = 0; e < 4; ++e) { w[e] = k0[rd][e]; w[4 + e] = i < 255 ? k1v[rd][e] : 0u; }
#pragma unroll
      for (int e = 0; e < 7; ++e) o[e] = __builtin_amdgcn_alignbit(w[e + 1], w[e], 16);
#pragma unroll
      for (int c = 0; c < 4; ++c) {
        sts128(kc + (2 * c) * KCS + i * 16, u32x4{w[c], w[c + 1], w[c + 2], w[c + 3]});
        sts128(kc + (2 * c + 1) * KCS + i * 16, u32x4{o[c], o[c + 1], o[c + 2], o[c + 3]});
      }
    }
    pair_sync(lb + L2_FL + cl * 4, 2u, lane);
    const int l0 = 16 * ((r >> 2) & 1) + 4 * (r >> 3) + (r & 3);
    const unsigned ka = kc + (7 - (l0 & 7)) * KCS + (2040 - 1024 - 32 * (16 * half + 15) - 8 * (l0 >> 3) + 8 * h) * 2;
    const unsigned ua = ui + (r + 31 - (16 * half + 15)) * 80 + h * 16;
    f32x16 ac0, ac1;
#pragma unroll
    for (int e = 0; e < 16; ++e) { ac0[e] = 0.f; ac1[e] = 0.f; }
#pragma unroll
    for (int it = 0; it < 16; ++it)
#pragma unroll
      for (int kb = 0; kb < 2; ++kb) {
        const bf16x8 ahi = __builtin_bit_cast(bf16x8, lds128(ka + it * 64 + kb * 32));
        const bf16x8 alo = __builtin_bit_cast(bf16x8, lds128(ka + it * 64 + kb * 32 + 2048));
        const bf16x8 blo = __builtin_bit_cast(bf16x8, lds128(ua + it * 80 + kb * 32));
        const bf16x8 bhi = __builtin_bit_cast(bf16x8, lds128(ua + it * 80 + kb * 32 + 32 * 80));
        ac1 = mfma32(alo, bhi, ac1);
        ac0 = mfma32(alo, blo, ac0);
        ac1 = mfma32(ahi, blo, ac1);
      }
    const f32x16 give = half ? ac0 : ac1;
    f32x16 keep = half ? ac1 : ac0;
#pragma unroll
    for (int q = 0; q < 4; ++q) {
      f32x4 v = {give[4 * q], give[4 * q + 1], give[4 * q + 2], give[4 * q + 3]};
      sts128(lb + L2_PX + cw * 4096 + q * 1024 + lane * 16, __builtin_bit_cast(u32x4, v));
    }
    pair_sync(lb + L2_FL + cl * 4, 4u, lane);
    const __amdgpu_buffer_rsrc_t ars = __builtin_amdgcn_make_buffer_rsrc(p.ws + OFF_AT, (short)0, SL * SD * 2, 0x00020000);
#pragma unroll
    for (int hh = 0; hh < 2; ++hh) {
      const u32x4 uq = lds128(ui + (32 * half + r + 31) * 80 + h * 32 + hh * 16);
      u32x4 o;
#pragma unroll
      for (int q2 = 0; q2 < 2; ++q2) {
        const int q = 2 * hh + q2;
        const f32x4 v = __builtin_bit_cast(f32x4, lds128(lb + L2_PX + (cw ^ 1) * 4096 + q * 1024 + lane * 16));
#pragma unroll
        for (int e2 = 0; e2 < 2; ++e2) {
          const unsigned uw = uq[2 * q2 + e2];
          const float y0 = rbf(keep[4 * q + 2 * e2] + v[2 * e2]), y1 = rbf(keep[4 * q + 2 * e2 + 1] + v[2 * e2 + 1]);
          o[2 * q2 + e2] = pk2(y0 + rbf(bf2f(uw) * dsk), y1 + rbf(bfhi(uw) * dsk));
        }
      }
      __builtin_amdgcn_raw_buffer_store_b128(o, ars, (ch * SL + 32 * (32 * half + r) + 16 * h + 8 * hh) * 2, 0, 16);
    }
    asm volatile("s_waitcnt vmcnt(0)" ::: "memory");
    unsigned old = 0;
    if (lane == 0) old = __hip_atomic_fetch_add((LAS unsigned*)(uintptr_t)(lb + L2_FL + 16), 1u, __ATOMIC_RELAXED, __HIP_MEMORY_SCOPE_WORKGROUP);
    old = __builtin_amdgcn_readfirstlane(old);
    if (old == 3u) {
      GAS unsigned* cnt = (GAS unsigned*)(p.ws + OFF_CNT);
      if (lane == 0) __hip_atomic_fetch_add(cnt + (b >> 5) * 32, 1u, __ATOMIC_RELAXED, __HIP_MEMORY_SCOPE_AGENT);
      for (int spin = 0; spin < (1 << 22); ++spin) {
        if (__hip_atomic_load(cnt + nt * 32, __ATOMIC_RELAXED, __HIP_MEMORY_SCOPE_AGENT) >= 32u) break;
        __builtin_amdgcn_s_sleep(4);
      }
      asm volatile("" ::: "memory");
      u32x4 av[8];
#pragma unroll
      for (int i = 0; i < 8; ++i)
        av[i] = __builtin_amdgcn_raw_buffer_load_b128(ars, ((nt * 64 + 8 * i + (lane >> 3)) * SL + mt * 64 + (lane & 7) * 8) * 2, 0, 16);
#pragma unroll
      for (int i = 0; i < 8; ++i) sts128(lb + L2_AT + (8 * i + (lane >> 3)) * ATS + (lane & 7) * 16, av[i]);
    }
    bar();
  }
  bar();
  {
    const unsigned aa = lb + L2_AT + (8 * fo + ((lane & 15) >> 2)) * ATS + (16 * (wv & 3) + (lane & 3) * 4) * 2;
    const u32x4 av = __builtin_bit_cast(u32x4, trfrag(aa, aa + 4 * ATS));
    f32x4 s0 = {0.f, 0.f, 0.f, 0.f}, s1 = s0;
#pragma unroll
    for (int w = 0; w < 4; ++w) {
      s0 += __builtin_bit_cast(f32x4, lds128(lb + (w * 64 + ftok) * RS + fo * 32));
      s1 += __builtin_bit_cast(f32x4, lds128(lb + (w * 64 + ftok) * RS + fo * 32 + 16));
    }
    const float og = rbf(1.f - gate);
    u32x4 o;
#pragma unroll
    for (int e = 0; e < 4; ++e) {
      const float b0 = e < 2 ? s0[2 * e] : s1[2 * e - 4], b1 = e < 2 ? s0[2 * e + 1] : s1[2 * e - 3];
      o[e] = pk2(rbf(gate * bf2f(av[e])) + rbf(og * rbf(b0)), rbf(gate * bfhi(av[e])) + rbf(og * rbf(b1)));
    }
    *(u32x4*)(p.out + (size_t)(mt * 64 + ftok) * SD + nt * 64 + fo * 8) = o;
  }
}

extern "C" void kernel_launch(void* const* d_in, const int* in_sizes, int n_in, void* d_out, int out_size, void* d_ws,
                              size_t ws_size, hipStream_t stream) {
  P1 a;
  a.x = (const u16*)d_in[0]; a.are = (const u16*)d_in[1]; a.aim = (const u16*)d_in[2]; a.bre = (const u16*)d_in[3];
  a.bim = (const u16*)d_in[4]; a.cre = (const u16*)d_in[5]; a.cim = (const u16*)d_in[6]; a.ldt = (const u16*)d_in[7];
  a.w1 = (const u16*)d_in[10]; a.w3 = (const u16*)d_in[11]; a.w2 = (const u16*)d_in[12]; a.gw = (const u16*)d_in[13];
  a.ws = (char*)d_ws;
  P2 c;
  c.dskip = (const u16*)d_in[8]; c.out = (u16*)d_out; c.ws = (char*)d_ws;
  (void)hipFuncSetAttribute((const void*)k1, hipFuncAttributeMaxDynamicSharedMemorySize, L1_TOTAL);
  (void)hipFuncSetAttribute((const void*)k2, hipFuncAttributeMaxDynamicSharedMemorySize, L2_TOTAL);
  k1<<<256, 512, L1_TOTAL, stream>>>(a);
  k2<<<256, 512, L2_TOTAL, stream>>>(c);
}
```

```cpp
#include <hip/hip_runtime.h>
#include <stdint.h>

typedef unsigned short u16;
typedef __bf16 bf16x8 __attribute__((ext_vector_type(8)));
typedef __bf16 bf16x2 __attribute__((ext_vector_type(2)));
typedef short s16x4 __attribute__((ext_vector_type(4)));
typedef float f32x2 __attribute__((ext_vector_type(2)));
typedef float f32x4 __attribute__((ext_vector_type(4)));
typedef float f32x16 __attribute__((ext_vector_type(16)));
typedef unsigned u32x2 __attribute__((ext_vector_type(2)));
typedef unsigned u32x4 __attribute__((ext_vector_type(4)));
#define LAS __attribute__((address_space(3)))
#define GAS __attribute__((address_space(1)))
#define DI __device__ __forceinline__

constexpr int SL = 2048, SD = 512, SN = 128, SF = 1792;
constexpr size_t OFF_HT = 0;
constexpr size_t OFF_W2T = OFF_HT + (size_t)SL * SF * 2;
constexpr size_t OFF_UT = OFF_W2T + (size_t)SF * SD * 2;
constexpr size_t OFF_KR = OFF_UT + (size_t)SL * SD * 2;
constexpr size_t OFF_AT = OFF_KR + (size_t)SL * SD * 2;
constexpr size_t OFF_G = OFF_AT + (size_t)SL * SD * 2;
constexpr size_t OFF_CNT = OFF_G + (size_t)SL * 4;

#define ST(K, J)
#define STDECL
DI float bf2f(unsigned v) { return __builtin_bit_cast(float, v << 16); }
DI float bfhi(unsigned v) { return __builtin_bit_cast(float, v & 0xffff0000u); }
DI unsigned pk2(float a, float b) { f32x2 v = {a, b}; return __builtin_bit_cast(unsigned, __builtin_convertvector(v, bf16x2)); }
DI float rbf(float a) { return bf2f(pk2(a, 0.f) & 0xffffu); }
DI void lgkm0() { asm volatile("s_waitcnt lgkmcnt(0)" ::: "memory"); }
DI void bar() { asm volatile("s_waitcnt lgkmcnt(0)\n\ts_barrier" ::: "memory"); }
DI void glds16(const void* sbase, unsigned voff, unsigned lds) {
  asm volatile("s_mov_b32 m0, %0\n\ts_nop 0\n\tglobal_load_lds_dwordx4 %1, %2" ::"s"(lds), "v"(voff), "s"(sbase) : "memory");
}
DI u32x4 lds128(unsigned a) { return *(const LAS u32x4*)(uintptr_t)a; }
DI void sts128(unsigned a, u32x4 v) { *(LAS u32x4*)(uintptr_t)a = v; }
DI bf16x8 trfrag(unsigned a0, unsigned a1) {
  s16x4 lo = __builtin_amdgcn_ds_read_tr16_b64_v4i16((LAS s16x4*)(uintptr_t)a0);
  s16x4 hi = __builtin_amdgcn_ds_read_tr16_b64_v4i16((LAS s16x4*)(uintptr_t)a1);
  u32x2 l = __builtin_bit_cast(u32x2, lo), h = __builtin_bit_cast(u32x2, hi);
  u32x4 r = {l.x, l.y, h.x, h.y};
  return __builtin_bit_cast(bf16x8, r);
}
#define WT 0
DI void st16(void* a, bf16x8 v) {
  *(bf16x8*)a = v;
}
DI f32x16 mfma32(bf16x8 a, bf16x8 b, f32x16 c) { return __builtin_amdgcn_mfma_f32_32x32x16_bf16(a, b, c, 0, 0, 0); }
DI f32x4 mfma16(bf16x8 a, bf16x8 b, f32x4 c) { return __builtin_amdgcn_mfma_f32_16x16x32_bf16(a, b, c, 0, 0, 0); }

struct P1 {
  const u16 *x, *are, *aim, *bre, *bim, *cre, *cim, *ldt, *w1, *w3, *w2, *gw;
  char* ws;
};

constexpr unsigned STG = 49152, L1_PAR = 3 * STG, L1_KT = L1_PAR + 4096, L1_TOTAL = L1_KT + 12288;

#define SB0 __builtin_amdgcn_sched_barrier(0)
DI f32x2 cmul(f32x2 a, f32x2 b) { return f32x2{a.x, a.x} * b + f32x2{-a.y, a.y} * f32x2{b.y, b.x}; }
DI f32x2 cexp(float a, float rev) {
  rev = __builtin_amdgcn_fractf(rev);
  return f32x2{__builtin_amdgcn_cosf(rev), __builtin_amdgcn_sinf(rev)} * __builtin_amdgcn_exp2f(a);
}
DI void kgen_table(const P1& p, int ch0, int t, unsigned lb) {
  const int ch = ch0 + (t >> 7), o = ch * SN + (t & 127);
  const float ar = bf2f(p.are[o]), ai = bf2f(p.aim[o]), br = bf2f(p.bre[o]), bi = bf2f(p.bim[o]);
  const float cr = bf2f(p.cre[o]), ci = bf2f(p.cim[o]);
  const float dt = rbf(expf(bf2f(p.ldt[ch])));
  const float dbr = dt * br, dbi = dt * bi;
  const float a = (dt * ar) * 1.4426950408889634f, rev = (dt * ai) * 0.15915494309189535f;
  f32x2 e[16], bq[16], gq[9];
  e[0] = f32x2{1.f, 0.f}; e[1] = cexp(a, rev);
  const f32x2 c16 = cexp(16.f * a, -16.f * rev);
  gq[1] = cexp(256.f * a, 256.f * rev);
  bq[0] = f32x2{cr * dbr - ci * dbi, -(cr * dbi + ci * dbr)};
  const f32x2 c32 = cmul(c16, c16), c64 = cmul(c32, c32);
  bq[1] = cmul(bq[0], c16); bq[2] = cmul(bq[0], c32); bq[3] = cmul(bq[1], c32);
#pragma unroll
  for (int j = 2; j < 16; ++j) e[j] = cmul(e[j >> 1], e[j - (j >> 1)]);
#pragma unroll
  for (int j = 2; j < 9; ++j) gq[j] = cmul(gq[j >> 1], gq[j - (j >> 1)]);
#pragma unroll
  for (int j = 4; j < 16; ++j) bq[j] = cmul(bq[j - 4], c64);
  const unsigned base = lb + 2 * STG + (t >> 6) * 4096 + (t & 63) * 4;
#pragma unroll
  for (int r = 0; r < 16; ++r) {
    LAS unsigned* w = (LAS unsigned*)(uintptr_t)((base ^ (r << 4)) + r * 256);
    const f32x2 v1 = r < 8 ? (r ? cmul(gq[1], e[2 * r]) : gq[1]) : ((r & 3) ? cmul(gq[r >> 2], e[4 * (r & 3)]) : gq[r >> 2]);
    w[0] = pk2(bq[r].x, bq[r].y);
    w[4096] = pk2(e[r].x, e[r].y);
    w[8192] = pk2(v1.x, v1.y);
    if (r <= 8) { const f32x2 v2 = (r & 1) ? cmul(gq[4 + (r >> 1)], e[8]) : gq[4 + (r >> 1)]; w[12288] = pk2(v2.x, v2.y); }
    else w[12288] = 0u;
  }
}
struct KG { f32x4 acc[3]; bf16x8 f[4][4]; };
DI void kgen_load(KG& k, int hw, int lane, unsigned lb) {
  const int r = lane & 15;
#pragma unroll
  for (int ks = 0; ks < 4; ++ks)
#pragma unroll
    for (int t = 0; t < 4; ++t)
      k.f[ks][t] = __builtin_bit_cast(bf16x8, lds128(lb + 2 * STG + t * 16384 + hw * 4096 + r * 256 + (((ks * 4 + (lane >> 4)) ^ r) << 4)));
}
DI void kgen_mma(KG& k) {
#pragma unroll
  for (int t = 0; t < 3; ++t) k.acc[t] = f32x4{0.f, 0.f, 0.f, 0.f};
#pragma unroll
  for (int ks = 0; ks < 4; ++ks)
#pragma unroll
    for (int t = 0; t < 3; ++t) k.acc[t] = mfma16(k.f[ks][t + 1], k.f[ks][0], k.acc[t]);
}
DI void kgen_put(const KG& k, int hw, int lane, unsigned lb) {
  LAS float* kt = (LAS float*)(uintptr_t)(lb + L1_KT + (hw >> 1) * 6144 + (hw & 1) * 4);
  const int g = lane >> 4, j = lane & 15;
  const int c1 = g < 2 ? 256 + 4 * g + 8 * j : 256 + 64 * g + 4 * j;
#pragma unroll
  for (int e = 0; e < 4; ++e) {
    kt[(4 * g + e + 16 * j) * 2] = k.acc[0][e];
    kt[(c1 + e) * 2] = k.acc[1][e];
    const int c2 = g < 2 ? 512 + 64 * g + 32 * (e >> 1) + (e & 1) + 2 * j : 640 + 2 * j + ((g == 2 && e == 0) ? 0 : 40);
    kt[c2 * 2] = k.acc[2][e];
  }
}
DI void kgen_expand(const P1& p, int ch0, int hw, int lane, unsigned lb, int rd) {
  const unsigned kt = lb + L1_KT + (hw >> 1) * 6144;
  const int i = rd * 128 + (hw & 1) * 64 + lane, l0 = 2047 - 8 * i;
  const float lf = (float)l0;
  f32x2 v[8];
#pragma unroll
  for (int e = 0; e < 8; e += 2) {
    const unsigned w = pk2(lf - (float)e, lf - (float)(e + 1));
    const int c0 = l0 < 128 ? l0 - e : (int)(w & 0xffffu) - (133 << 7), c1 = l0 < 128 ? l0 - e - 1 : (int)(w >> 16) - (133 << 7);
    v[e] = *(const LAS f32x2*)(uintptr_t)(kt + c0 * 8);
    v[e + 1] = *(const LAS f32x2*)(uintptr_t)(kt + c1 * 8);
  }
  u32x4 o;
#pragma unroll
  for (int e = 0; e < 4; ++e) o[e] = pk2(v[2 * e].x + v[2 * e].y, v[2 * e + 1].x + v[2 * e + 1].y);
  st16((u16*)(p.ws + OFF_KR) + (size_t)(ch0 + (hw >> 1)) * SL + 8 * i, __builtin_bit_cast(bf16x8, o));
}

#define SGB(m, n) __builtin_amdgcn_sched_group_barrier(m, n, 0)
#define VMW(n) asm volatile("s_waitcnt vmcnt(" #n ")" ::: "memory")
#define MAP 0

__global__ __launch_bounds__(512, 2) void k1(P1 p) {
  extern __shared__ __attribute__((aligned(16))) char smem[];
  const unsigned lb = (unsigned)(uintptr_t)(LAS char*)smem;
  const int b = blockIdx.x, xcd = b & 7, jj = b >> 3, tid = threadIdx.x, lane = tid & 63;
  const int wv = __builtin_amdgcn_readfirstlane(tid >> 6);
  const int ch0 = 2 * b;
  const int g = lane >> 4, q = (lane & 15) >> 2, pp = lane & 3;
  STDECL;
  ST(0, 0);
  if (jj < 28) {
    const int mt = (xcd >> 1) * 4 + (jj & 3), ft = (xcd & 1) * 7 + (jj >> 2);
#define ROT1 0
    const int rot = ROT1, ti = ft < 8 ? (ft - rot) & 7 : -9;
    if (wv >= 4) {
      const int hw = wv - 4, un = mt * 14 + ft;
      const unsigned vx0 = (lane >> 3) * 1024 + (((lane & 7) ^ ((lane >> 4) & 7)) << 4);
      const unsigned vx1 = (lane >> 3) * 1024 + (((lane & 7) ^ ((4 + (lane >> 4)) & 7)) << 4);
      const unsigned vw1 = (lane >> 4) * (SF * 2) + (((lane & 15) ^ ((lane >> 4) << 2)) << 4);
      const unsigned vw3 = (lane >> 4) * (SF * 2) + (((lane & 15) ^ (((lane >> 4) << 2) | 2)) << 4);
      const char* xb = (const char*)p.x + (size_t)(mt * 128 + hw * 32) * 1024;
      const char* w1b = (const char*)p.w1 + (size_t)(hw * 16) * (SF * 2) + ft * 256;
      const char* w3b = (const char*)p.w3 + (size_t)(hw * 16) * (SF * 2) + ft * 256;
      auto issue = [&](int it) {
        const unsigned d = lb + (it % 3) * STG + hw * 4096;
        const int st = (it + rot) & 7;
#pragma unroll
        for (int j = 0; j < 4; ++j) {
          glds16(w1b + (size_t)(st * 64 + j * 4) * (SF * 2), vw1, d + 16384 + j * 1024);
          glds16(w3b + (size_t)(st * 64 + j * 4) * (SF * 2), vw3, d + 32768 + j * 1024);
          glds16(xb + j * 8192 + st * 128, (j & 1) ? vx1 : vx0, d + j * 1024);
        }
      };
      issue(0);
      issue(1);
      KG k;
#pragma unroll
      for (int i = 0; i < 8; ++i) {
        if (i == 2) { if (ti == 1) VMW(18); else VMW(14); }
        else if (i == 4) { if (ti == 3) VMW(18); else VMW(14); }
        else if (i == 6) { if (ti == 5) VMW(17); else VMW(13); }
        else if (i == 7) VMW(1);
        else { if (ti == i - 1) VMW(16); else VMW(12); }
        if (i < 4) ST(0, 1 + 3 * i);
        bar();
        if (i < 4) ST(0, 2 + 3 * i);
        if (i == 0) { kgen_load(k, hw, lane, lb); lgkm0(); }
        if (i == 1) {
#pragma unroll
          for (int j = 0; j < 2; ++j)
            glds16((const char*)p.w2 + (size_t)((un >> 1) * 16 + hw * 4 + j * 2) * 1024 + (un & 1) * 512,
                   (lane >> 5) * 1024 + (((lane & 31) ^ ((2 * j + (lane >> 5)) << 2)) << 4), lb + L1_KT + hw * 2048 + j * 1024);
        }
        if (i < 6) issue(i + 2);
        if (i < 4) ST(0, 3 + 3 * i);
        if (i == 0) kgen_mma(k);
        if (i == 3) {
#pragma unroll
          for (int j = 0; j < 2; ++j) {
            const int db = hw * 2 + j;
            const unsigned a = lb + L1_KT + (8 * (g >> 1) + q) * 512 + ((db ^ q) << 6) + (g & 1) * 32 + pp * 8;
            st16(p.ws + OFF_W2T + ((size_t)(((un & 1) * 8 + db) * 112 + (un >> 1)) << 10) + lane * 16, trfrag(a, a + 2048));
          }
        }
        if (ti == i) {
#pragma unroll
          for (int j = 0; j < 4; ++j) {
            const int r0 = j * 32 + g * 8 + q;
            const unsigned a0 = lb + (i % 3) * STG + r0 * 128 + (((2 * hw + (pp >> 1)) ^ ((r0 >> 1) & 7)) << 4) + (pp & 1) * 8;
            const unsigned a1 = lb + (i % 3) * STG + (r0 + 4) * 128 + (((2 * hw + (pp >> 1)) ^ (((r0 + 4) >> 1) & 7)) << 4) + (pp & 1) * 8;
            st16(p.ws + OFF_UT + ((size_t)(ft * 64 + hw * 16 + (lane & 15)) * SL + mt * 128 + j * 32 + g * 8) * 2, trfrag(a0, a1));
          }
        }
        if (i == 4) kgen_put(k, hw, lane, lb);
        if (i == 5 || i == 6) kgen_expand(p, ch0, hw, lane, lb, i - 5);
      }
    } else {
      kgen_table(p, ch0, tid, lb);
      const int wm = wv & 1, wn = wv >> 1, r = lane & 31, h = lane >> 5;
      unsigned xa[4], wa[4];
#pragma unroll
      for (int s = 0; s < 4; ++s) xa[s] = lb + (wm * 64 + r) * 128 + (((2 * s + h) ^ ((r >> 1) & 7)) << 4);
#pragma unroll
      for (int c = 0; c < 4; ++c)
        wa[c] = lb + 16384 + (g & 1) * 16384 + (8 * (g >> 1) + q) * 256 + (((4 * wn + c) ^ ((q << 1) | (g & 1))) << 5) +
                ((((pp & 1) << 1) | (pp >> 1)) << 3);
      f32x16 acc[2][4];
#pragma unroll
      for (int t = 0; t < 2; ++t)
#pragma unroll
        for (int c = 0; c < 4; ++c)
#pragma unroll
          for (int e = 0; e < 16; ++e) acc[t][c][e] = 0.f;
      bf16x8 xf[2][2], wf[2][4];
      auto ld = [&](int gs) {
        const unsigned so = ((gs >> 2) % 3) * STG;
        const int s = gs & 3, u = gs & 1;
        wf[u][0] = trfrag(wa[0] + so + s * 4096, wa[0] + so + s * 4096 + 1024);
        xf[u][0] = __builtin_bit_cast(bf16x8, lds128(xa[s] + so));
        xf[u][1] = __builtin_bit_cast(bf16x8, lds128(xa[s] + so + 4096));
#pragma unroll
        for (int c = 1; c < 4; ++c) wf[u][c] = trfrag(wa[c] + so + s * 4096, wa[c] + so + s * 4096 + 1024);
      };
      bar();
      ST(0, 1);
      ld(0);
#pragma unroll
      for (int gs = 0; gs < 32; ++gs) {
        SB0;
        if ((gs & 3) == 3 && gs < 31) { bar(); ST(0, 2 + (gs >> 2)); SB0; }
        if (gs < 31) ld(gs + 1);
#pragma unroll
        for (int c = 0; c < 4; ++c) {
          acc[0][c] = mfma32(wf[gs & 1][c], xf[gs & 1][0], acc[0][c]);
          acc[1][c] = mfma32(wf[gs & 1][c], xf[gs & 1][1], acc[1][c]);
        }
#pragma unroll
        for (int j = 0; j < 5; ++j) { SGB(0x8, 1); SGB(0x100, 2); }
        SGB(0x8, 3);
      }
      SB0;
      ST(0, 9);
#pragma unroll
      for (int t = 0; t < 2; ++t)
#pragma unroll
        for (int c = 0; c < 4; ++c) {
          u32x4 o;
#pragma unroll
          for (int e = 0; e < 4; ++e) {
            const f32x2 gt = {acc[t][c][2 * e], acc[t][c][2 * e + 1]}, up = {acc[t][c][8 + 2 * e], acc[t][c][9 + 2 * e]};
            const f32x2 z = gt * -1.4426950408889634f;
            f32x2 ex = {__builtin_amdgcn_exp2f(z.x), __builtin_amdgcn_exp2f(z.y)};
            ex = ex + 1.f;
            const f32x2 rc = {__builtin_amdgcn_rcpf(ex.x), __builtin_amdgcn_rcpf(ex.y)};
            const f32x2 hv = (gt * rc) * up;
            o[e] = pk2(hv.x, hv.y);
          }
          st16(p.ws + OFF_HT + ((size_t)((mt * 4 + wm * 2 + t) * 112 + ft * 8 + wn * 4 + c) << 10) + lane * 16, __builtin_bit_cast(bf16x8, o));
        }
    }
  } else {
    const int sp = xcd * 4 + (jj - 28);
    if (wv >= 4) {
      const int hw = wv - 4;
      KG k;
      bar();
      kgen_load(k, hw, lane, lb);
      kgen_mma(k);
      bar();
      kgen_put(k, hw, lane, lb);
      bar();
      kgen_expand(p, ch0, hw, lane, lb, 0);
      kgen_expand(p, ch0, hw, lane, lb, 1);
    } else {
      u32x4 uv[16];
#pragma unroll
      for (int i = 0; i < 16; ++i) uv[i] = *(const u32x4*)(p.x + (size_t)(sp * 64 + wv * 16 + i) * SD + lane * 8);
      const u32x4 gw = *(const u32x4*)(p.gw + lane * 8);
      kgen_table(p, ch0, tid, lb);
      bar();
      const u32x4 ones = {~0u, ~0u, ~0u, ~0u};
#pragma unroll
      for (int i = 0; i < 16; ++i) st16(p.ws + OFF_AT + (size_t)sp * 65536 + i * 4096 + tid * 16, __builtin_bit_cast(bf16x8, ones));
      bar();
      bar();
      float gsel = 0.f;
#pragma unroll
      for (int i = 0; i < 16; ++i) {
        float s = 0.f;
#pragma unroll
        for (int e = 0; e < 4; ++e) s += bf2f(uv[i][e]) * bf2f(gw[e]) + bfhi(uv[i][e]) * bfhi(gw[e]);
#pragma unroll
        for (int o = 32; o > 0; o >>= 1) s += __shfl_xor(s, o);
        if (lane == i) gsel = s;
      }
      if (lane < 16) ((float*)(p.ws + OFF_G))[sp * 64 + wv * 16 + lane] = rbf(1.f / (1.f + expf(-rbf(gsel))));
    }
  }
  ST(0, 15);
}

struct P2 { const u16* dskip; u16* out; char* ws; };
constexpr unsigned KCS = 4160, L2_UI = 2 * 8 * KCS, UIS = 4608, L2_PX = L2_UI + 2 * UIS, L2_AT = L2_PX + 16384, ATS = 144;
constexpr unsigned L2_FL = L2_AT + 64 * ATS, L2_TOTAL = L2_FL + 64, RS = 272;
static_assert(4 * 64 * RS <= L2_PX, "");
#define PF 6

DI void pair_sync(unsigned a, unsigned target, int lane) {
  lgkm0();
  LAS unsigned* f = (LAS unsigned*)(uintptr_t)a;
  if (lane == 0) __hip_atomic_fetch_add(f, 1u, __ATOMIC_RELAXED, __HIP_MEMORY_SCOPE_WORKGROUP);
  while (__hip_atomic_load(f, __ATOMIC_RELAXED, __HIP_MEMORY_SCOPE_WORKGROUP) < target) __builtin_amdgcn_s_sleep(1);
  asm volatile("" ::: "memory");
}

__global__ __launch_bounds__(512, 2) void k2(P2 p) {
  extern __shared__ __attribute__((aligned(16))) char smem[];
  const unsigned lb = (unsigned)(uintptr_t)(LAS char*)smem;
  const int b = blockIdx.x, xcd = b & 7, jj = b >> 3, tid = threadIdx.x, lane = tid & 63;
  const int wv = __builtin_amdgcn_readfirstlane(tid >> 6);
  const int mt = (xcd >> 1) * 8 + (jj & 7), nt = (xcd & 1) * 4 + (jj >> 3);
  const int r = lane & 31, h = lane >> 5;
  const int ftok = 16 * (wv & 3) + (lane & 15), fo = 4 * (wv >> 2) + (lane >> 4);
  STDECL;
  ST(1, 0);
  if (tid < 16) *(LAS unsigned*)(uintptr_t)(lb + L2_FL + tid * 4) = 0u;
  const float gate = ((const float*)(p.ws + OFF_G))[mt * 64 + ftok];
  if (wv < 4) {
    const char* wa = p.ws + OFF_W2T + ((size_t)(2 * nt * 112 + 28 * wv) << 10);
    const char* hb = p.ws + OFF_HT + ((size_t)(2 * mt * 112 + 28 * wv) << 10);
#define ROT2 0
    int kq = (ROT2) % 28;
    bf16x8 A[PF][2], B[PF][2];
    auto ldg = [&](int u) {
      const unsigned o = kq * 1024 + lane * 16;
      B[u][0] = *(const bf16x8*)(hb + o); A[u][0] = *(const bf16x8*)(wa + o);
      B[u][1] = *(const bf16x8*)(hb + 112 * 1024 + o); A[u][1] = *(const bf16x8*)(wa + 112 * 1024 + o);
      kq = kq == 27 ? 0 : kq + 1;
    };
    bar();
#pragma unroll
    for (int s = 0; s < PF; ++s) ldg(s);
    SB0;
    f32x16 acc[2][2];
#pragma unroll
    for (int a = 0; a < 2; ++a)
#pragma unroll
      for (int t = 0; t < 2; ++t)
#pragma unroll
        for (int e = 0; e < 16; ++e) acc[a][t][e] = 0.f;
#define NS 28
#pragma unroll
    for (int s = 0; s < NS; ++s) {
      const int u = s % PF;
      acc[0][0] = mfma32(A[u][0], B[u][0], acc[0][0]);
      acc[0][1] = mfma32(A[u][0], B[u][1], acc[0][1]);
      acc[1][0] = mfma32(A[u][1], B[u][0], acc[1][0]);
      acc[1][1] = mfma32(A[u][1], B[u][1], acc[1][1]);
      if (s + PF < NS) ldg(u);
      SB0;
    }
    ST(1, 1);
    bar();
    ST(1, 2);
#pragma unroll
    for (int a = 0; a < 2; ++a)
#pragma unroll
      for (int t = 0; t < 2; ++t)
#pragma unroll
        for (int q = 0; q < 4; ++q) {
          f32x4 v = {acc[a][t][4 * q], acc[a][t][4 * q + 1], acc[a][t][4 * q + 2], acc[a][t][4 * q + 3]};
          sts128(lb + (wv * 64 + 32 * t + r) * RS + (32 * a + 8 * q + 4 * h) * 4, __builtin_bit_cast(u32x4, v));
        }
  } else {
    const int cw = wv - 4, cl = cw >> 1, half = cw & 1, ch = 2 * b + cl;
    const u16* kr = (const u16*)(p.ws + OFF_KR) + (size_t)ch * SL;
    const u16* ut = (const u16*)(p.ws + OFF_UT) + (size_t)ch * SL;
    const unsigned kc = lb + cl * 8 * KCS, ui = lb + L2_UI + cl * UIS;
    u32x4 k0[2], k1v[2], uu[2];
#pragma unroll
    for (int rd = 0; rd < 2; ++rd) {
      const int i = rd * 128 + half * 64 + lane;
      k0[rd] = *(const u32x4*)(kr + 8 * i);
      k1v[rd] = *(const u32x4*)(kr + 8 * (i < 255 ? i + 1 : i));
      uu[rd] = *(const u32x4*)(ut + 8 * i);
    }
    const float dsk = bf2f(p.dskip[ch]);
    bar();
    const u32x4 z4 = {0u, 0u, 0u, 0u};
    if (lane < 32) sts128(half ? ui + lane * 16 : kc + (lane >> 2) * KCS + (256 + (lane & 3)) * 16, z4);
#pragma unroll
    for (int rd = 0; rd < 2; ++rd) {
      const int i = rd * 128 + half * 64 + lane;
      sts128(ui + 512 + i * 16, uu[rd]);
      unsigned w[8], o[7];
#pragma unroll
      for (int e = 0; e < 4; ++e) { w[e] = k0[rd][e]; w[4 + e] = i < 255 ? k1v[rd][e] : 0u; }
#pragma unroll
      for (int e = 0; e < 7; ++e) o[e] = __builtin_amdgcn_alignbit(w[e + 1], w[e], 16);
#pragma unroll
      for (int c = 0; c < 4; ++c) {
        sts128(kc + (2 * c) * KCS + i * 16, u32x4{w[c], w[c + 1], w[c + 2], w[c + 3]});
        sts128(kc + (2 * c + 1) * KCS + i * 16, u32x4{o[c], o[c + 1], o[c + 2], o[c + 3]});
      }
    }
    pair_sync(lb + L2_FL + cl * 4, 2u, lane);
    ST(1, 1);
    const int m = lane & 15, q = lane >> 4;
    const unsigned ka = kc + ((7 - m) & 7) * KCS + (2024 - 8 * (m >> 3) + 8 * q - 32 * (56 + 4 * half + 3)) * 2;
    const unsigned ua = ui + 512 + (16 * m + 8 * q - 16 - 32 * (4 * half + 3)) * 2;
    f32x4 ac[8];
#pragma unroll
    for (int e = 0; e < 8; ++e) ac[e] = f32x4{0.f, 0.f, 0.f, 0.f};
    bf16x8 fa[2][8], fb[2][8];
    auto ldc = [&](int it) {
#pragma unroll
      for (int i = 0; i < 8; ++i) {
        fa[it & 1][i] = __builtin_bit_cast(bf16x8, lds128(ka + it * 64 + (7 - i) * 512));
        fb[it & 1][i] = __builtin_bit_cast(bf16x8, lds128(ua + it * 64 + i * 512));
      }
    };
    ldc(0);
#pragma unroll
    for (int it = 0; it < 4; ++it) {
      SB0;
      if (it < 3) ldc(it + 1);
#pragma unroll
      for (int d = 0; d < 8; ++d)
#pragma unroll
        for (int i = 0; i <= d; ++i) {
          if (i + d < 8) ac[i + d] = mfma16(fa[it & 1][i], fb[it & 1][d], ac[i + d]);
          if (i < d && i + d < 8) ac[i + d] = mfma16(fa[it & 1][d], fb[it & 1][i], ac[i + d]);
        }
#pragma unroll
      for (int j = 0; j < 16; ++j) { SGB(0x8, 2); SGB(0x100, 1); }
      SGB(0x8, 4);
    }
    SB0;
    if (half) {
#pragma unroll
      for (int j = 0; j < 4; ++j) { const f32x4 t = ac[j]; ac[j] = ac[4 + j]; ac[4 + j] = t; asm volatile("" : "+v"(ac[j]), "+v"(ac[4 + j])); }
    }
#pragma unroll
    for (int j = 0; j < 4; ++j) sts128(lb + L2_PX + cw * 4096 + j * 1024 + lane * 16, __builtin_bit_cast(u32x4, ac[4 + j]));
    pair_sync(lb + L2_FL + cl * 4, 4u, lane);
    ST(1, 2);
    __builtin_amdgcn_s_setprio(3);
    const __amdgpu_buffer_rsrc_t ars = __builtin_amdgcn_make_buffer_rsrc(p.ws + OFF_AT, (short)0, SL * SD * 2, 0x00020000);
#pragma unroll
    for (int j = 0; j < 4; ++j) {
      const int l = 256 * (4 * half + j) + 16 * m + 4 * q;
      const u32x2 uq = *(const LAS u32x2*)(uintptr_t)(ui + 512 + l * 2);
      const f32x4 v = __builtin_bit_cast(f32x4, lds128(lb + L2_PX + (cw ^ 1) * 4096 + j * 1024 + lane * 16)) + ac[j];
      u32x2 o;
#pragma unroll
      for (int e = 0; e < 2; ++e) {
        const unsigned y = pk2(v[2 * e], v[2 * e + 1]), sk = pk2(bf2f(uq[e]) * dsk, bfhi(uq[e]) * dsk);
        const unsigned w = pk2(bf2f(y) + bf2f(sk), bfhi(y) + bfhi(sk));
        o[e] = w == ~0u ? 0x7fc07fc0u : w;
      }
      __builtin_amdgcn_raw_buffer_store_b64(o, ars, (ch * SL + l) * 2, 0, 16);
    }
    ST(1, 3);
    u32x4 av[2];
#pragma unroll 1
    for (int spin = 0; spin < (1 << 20); ++spin) {
      bool bad = false;
#pragma unroll
      for (int i = 0; i < 2; ++i) {
        av[i] = __builtin_amdgcn_raw_buffer_load_b128(ars, ((nt * 64 + 16 * cw + 8 * i + (lane >> 3)) * SL + mt * 64 + (lane & 7) * 8) * 2, 0, 16);
        bad |= av[i].x == ~0u || av[i].y == ~0u || av[i].z == ~0u || av[i].w == ~0u;
      }
      if (!__any(bad)) break;
      __builtin_amdgcn_s_sleep(8);
      asm volatile("" ::: "memory");
    }
#pragma unroll
    for (int i = 0; i < 2; ++i) sts128(lb + L2_AT + (16 * cw + 8 * i + (lane >> 3)) * ATS + (lane & 7) * 16, av[i]);
    ST(1, 5);
    bar();
  }
  bar();
  ST(1, 14);
  {
    const unsigned aa = lb + L2_AT + (8 * fo + ((lane & 15) >> 2)) * ATS + (16 * (wv & 3) + (lane & 3) * 4) * 2;
    const u32x4 av = __builtin_bit_cast(u32x4, trfrag(aa, aa + 4 * ATS));
    f32x4 s0 = {0.f, 0.f, 0.f, 0.f}, s1 = s0;
#pragma unroll
    for (int w = 0; w < 4; ++w) {
      s0 += __builtin_bit_cast(f32x4, lds128(lb + (w * 64 + ftok) * RS + fo * 32));
      s1 += __builtin_bit_cast(f32x4, lds128(lb + (w * 64 + ftok) * RS + fo * 32 + 16));
    }
    const float og = rbf(1.f - gate);
    u32x4 o;
#pragma unroll
    for (int e = 0; e < 4; ++e) {
      const float b0 = e < 2 ? s0[2 * e] : s1[2 * e - 4], b1 = e < 2 ? s0[2 * e + 1] : s1[2 * e - 3];
      o[e] = pk2(rbf(gate * bf2f(av[e])) + rbf(og * rbf(b0)), rbf(gate * bfhi(av[e])) + rbf(og * rbf(b1)));
    }
    *(u32x4*)(p.out + (size_t)(mt * 64 + ftok) * SD + nt * 64 + fo * 8) = o;
  }
  ST(1, 15);
}

extern "C" void kernel_launch(void* const* d_in, const int* in_sizes, int n_in, void* d_out, int out_size, void* d_ws,
                              size_t ws_size, hipStream_t stream) {
  P1 a;
  a.x = (const u16*)d_in[0]; a.are = (const u16*)d_in[1]; a.aim = (const u16*)d_in[2]; a.bre = (const u16*)d_in[3];
  a.bim = (const u16*)d_in[4]; a.cre = (const u16*)d_in[5]; a.cim = (const u16*)d_in[6]; a.ldt = (const u16*)d_in[7];
  a.w1 = (const u16*)d_in[10]; a.w3 = (const u16*)d_in[11]; a.w2 = (const u16*)d_in[12]; a.gw = (const u16*)d_in[13];
  a.ws = (char*)d_ws;
  P2 c;
  c.dskip = (const u16*)d_in[8]; c.out = (u16*)d_out; c.ws = (char*)d_ws;
  (void)hipFuncSetAttribute((const void*)k1, hipFuncAttributeMaxDynamicSharedMemorySize, L1_TOTAL);
  (void)hipFuncSetAttribute((const void*)k2, hipFuncAttributeMaxDynamicSharedMemorySize, L2_TOTAL);
  k1<<<256, 512, L1_TOTAL, stream>>>(a);
  k2<<<256, 512, L2_TOTAL, stream>>>(c);
}
```

```cpp
#include <hip/hip_runtime.h>
#include <stdint.h>

typedef unsigned short u16;
typedef __bf16 bf16x8 __attribute__((ext_vector_type(8)));
typedef __bf16 bf16x2 __attribute__((ext_vector_type(2)));
typedef short s16x4 __attribute__((ext_vector_type(4)));
typedef float f32x2 __attribute__((ext_vector_type(2)));
typedef float f32x4 __attribute__((ext_vector_type(4)));
typedef float f32x16 __attribute__((ext_vector_type(16)));
typedef unsigned u32x2 __attribute__((ext_vector_type(2)));
typedef unsigned u32x4 __attribute__((ext_vector_type(4)));
#define LAS __attribute__((address_space(3)))
#define GAS __attribute__((address_space(1)))
#define DI __device__ __forceinline__

constexpr int SL = 2048, SD = 512, SN = 128, SF = 1792;
constexpr size_t OFF_HT = 0;
constexpr size_t OFF_W2T = OFF_HT + (size_t)SL * SF * 2;
constexpr size_t OFF_UT = OFF_W2T + (size_t)SF * SD * 2;
constexpr size_t OFF_AT = OFF_UT + (size_t)SL * SD * 2;
constexpr size_t OFF_G = OFF_AT + (size_t)SL * SD * 2;

#define ST(K, J)
#define STDECL
DI float bf2f(unsigned v) { return __builtin_bit_cast(float, v << 16); }
DI float bfhi(unsigned v) { return __builtin_bit_cast(float, v & 0xffff0000u); }
DI unsigned pk2(float a, float b) { f32x2 v = {a, b}; return __builtin_bit_cast(unsigned, __builtin_convertvector(v, bf16x2)); }
DI float rbf(float a) { return bf2f(pk2(a, 0.f) & 0xffffu); }
DI void lgkm0() { asm volatile("s_waitcnt lgkmcnt(0)" ::: "memory"); }
DI void bar() { asm volatile("s_waitcnt lgkmcnt(0)\n\ts_barrier" ::: "memory"); }
DI void glds16(const void* sbase, unsigned voff, unsigned lds) {
  asm volatile("s_mov_b32 m0, %0\n\ts_nop 0\n\tglobal_load_lds_dwordx4 %1, %2" ::"s"(lds), "v"(voff), "s"(sbase) : "memory");
}
DI u32x4 lds128(unsigned a) { return *(const LAS u32x4*)(uintptr_t)a; }
DI void sts128(unsigned a, u32x4 v) { *(LAS u32x4*)(uintptr_t)a = v; }
DI bf16x8 trfrag(unsigned a0, unsigned a1) {
  s16x4 lo = __builtin_amdgcn_ds_read_tr16_b64_v4i16((LAS s16x4*)(uintptr_t)a0);
  s16x4 hi = __builtin_amdgcn_ds_read_tr16_b64_v4i16((LAS s16x4*)(uintptr_t)a1);
  u32x2 l = __builtin_bit_cast(u32x2, lo), h = __builtin_bit_cast(u32x2, hi);
  u32x4 r = {l.x, l.y, h.x, h.y};
  return __builtin_bit_cast(bf16x8, r);
}
#define WT 1
DI void st16(void* a, bf16x8 v) {
  asm volatile("global_store_dwordx4 %0, %1, off sc0 sc1\n\ts_nop 1" ::"v"(a), "v"(v) : "memory");
}
DI f32x16 mfma32(bf16x8 a, bf16x8 b, f32x16 c) { return __builtin_amdgcn_mfma_f32_32x32x16_bf16(a, b, c, 0, 0, 0); }
DI f32x4 mfma16(bf16x8 a, bf16x8 b, f32x4 c) { return __builtin_amdgcn_mfma_f32_16x16x32_bf16(a, b, c, 0, 0, 0); }

struct P1 {
  const u16 *x, *w1, *w3, *w2, *gw;
  char* ws;
};
struct P2 {
  const u16 *are, *aim, *bre, *bim, *cre, *cim, *ldt, *dskip;
  u16* out;
  char* ws;
};

constexpr unsigned STG = 49152, L1_W2 = 3 * STG, L1_TOTAL = L1_W2 + 8192;

#define SB0 __builtin_amdgcn_sched_barrier(0)
struct cf { float x, y; };
DI cf cmul(cf a, cf b) {
  cf r;
  asm("v_mul_f32 %0, %2, %4\n\tv_mul_f32 %1, %2, %5\n\tv_fma_f32 %0, -%3, %5, %0\n\tv_fma_f32 %1, %3, %4, %1"
      : "=&v"(r.x), "=&v"(r.y) : "v"(a.x), "v"(a.y), "v"(b.x), "v"(b.y));
  return r;
}
DI cf cexp(float a, float rev) {
  rev = __builtin_amdgcn_fractf(rev);
  const float m = __builtin_amdgcn_exp2f(a);
  cf r = {__builtin_amdgcn_cosf(rev) * m, __builtin_amdgcn_sinf(rev) * m};
  return r;
}
DI void kgen_table(const unsigned (&q)[7], int lane, unsigned tb) {
  const float dt = rbf(expf(bf2f(q[6])));
  const float dbr = dt * bf2f(q[2]), dbi = dt * bf2f(q[3]), cr = bf2f(q[4]), ci = bf2f(q[5]);
  const float a = (dt * bf2f(q[0])) * 1.4426950408889634f, rev = (dt * bf2f(q[1])) * 0.15915494309189535f;
  cf e[16], bq[16], gq[9];
  e[0] = cf{1.f, 0.f}; e[1] = cexp(a, rev);
  const cf c16 = cexp(16.f * a, -16.f * rev);
  gq[1] = cexp(256.f * a, 256.f * rev);
  bq[0] = cf{cr * dbr - ci * dbi, -(cr * dbi + ci * dbr)};
  const cf c32 = cmul(c16, c16), c64 = cmul(c32, c32);
  bq[1] = cmul(bq[0], c16); bq[2] = cmul(bq[0], c32); bq[3] = cmul(bq[1], c32);
#pragma unroll
  for (int j = 2; j < 16; ++j) e[j] = cmul(e[j >> 1], e[j - (j >> 1)]);
#pragma unroll
  for (int j = 2; j < 9; ++j) gq[j] = cmul(gq[j >> 1], gq[j - (j >> 1)]);
#pragma unroll
  for (int j = 4; j < 16; ++j) bq[j] = cmul(bq[j - 4], c64);
#pragma unroll
  for (int r = 0; r < 16; ++r) {
    LAS unsigned* w = (LAS unsigned*)(uintptr_t)(((tb + lane * 4) ^ (r << 4)) + r * 256);
    const cf v1 = r < 8 ? (r ? cmul(gq[1], e[2 * r]) : gq[1]) : ((r & 3) ? cmul(gq[r >> 2], e[4 * (r & 3)]) : gq[r >> 2]);
    w[0] = pk2(bq[r].x, bq[r].y);
    w[1024] = pk2(e[r].x, e[r].y);
    w[2048] = pk2(v1.x, v1.y);
    if (r <= 8) { const cf v2 = (r & 1) ? cmul(gq[4 + (r >> 1)], e[8]) : gq[4 + (r >> 1)]; w[3072] = pk2(v2.x, v2.y); }
    else w[3072] = 0u;
  }
}
DI void kgen_mma_put(int lane, unsigned tb, unsigned ktb) {
  const int j0 = (lane >> 4) * 4, r = lane & 15;
  bf16x8 f[4][4];
  f32x4 acc[3];
#pragma unroll
  for (int ks = 0; ks < 4; ++ks)
#pragma unroll
    for (int t = 0; t < 4; ++t) f[ks][t] = __builtin_bit_cast(bf16x8, lds128(tb + t * 4096 + r * 256 + (((ks * 4 + (lane >> 4)) ^ r) << 4)));
  SB0;
#pragma unroll
  for (int t = 0; t < 3; ++t) acc[t] = f32x4{0.f, 0.f, 0.f, 0.f};
#pragma unroll
  for (int ks = 0; ks < 4; ++ks)
#pragma unroll
    for (int t = 0; t < 3; ++t) acc[t] = mfma16(f[ks][0], f[ks][t + 1], acc[t]);
  LAS float* kt = (LAS float*)(uintptr_t)ktb;
  const int c1 = r < 8 ? 256 + r + 8 * j0 : 256 + 64 * (r >> 2) + (r & 3) + 4 * j0, s1 = r < 8 ? 8 : 4;
  const int c2 = (r <= 8 ? 512 + 32 * (r >> 1) + (r & 1) : 680) + 2 * j0;
#pragma unroll
  for (int e = 0; e < 4; ++e) {
    kt[(r + 16 * (j0 + e)) * 2] = acc[0][e];
    kt[(c1 + s1 * e) * 2] = acc[1][e];
    kt[(c2 + 2 * e) * 2] = acc[2][e];
  }
}

#define SGB(m, n) __builtin_amdgcn_sched_group_barrier(m, n, 0)
#define VMW(n) asm volatile("s_waitcnt vmcnt(" #n ")" ::: "memory")
#define MAP 0

__global__ __launch_bounds__(512, 2) void k1(P1 p) {
  extern __shared__ __attribute__((aligned(16))) char smem[];
  const unsigned lb = (unsigned)(uintptr_t)(LAS char*)smem;
  const int b = blockIdx.x, xcd = b & 7, jj = b >> 3, tid = threadIdx.x, lane = tid & 63;
  const int wv = __builtin_amdgcn_readfirstlane(tid >> 6);
  const int g = lane >> 4, q = (lane & 15) >> 2, pp = lane & 3;
  STDECL;
  ST(0, 0);
  if (jj < 28) {
    const int mt = (xcd >> 1) * 4 + (jj & 3), ft = (xcd & 1) * 7 + (jj >> 2);
#define ROT1 0
    const int rot = ROT1, ti = ft < 8 ? (ft - rot) & 7 : -9;
      const int hw = wv & 3, un = mt * 14 + ft;
      const unsigned vx0 = (lane >> 3) * 1024 + (((lane & 7) ^ ((lane >> 4) & 7)) << 4);
      const unsigned vx1 = (lane >> 3) * 1024 + (((lane & 7) ^ ((4 + (lane >> 4)) & 7)) << 4);
      const unsigned vw1 = (lane >> 4) * (SF * 2) + (((lane & 15) ^ ((lane >> 4) << 2)) << 4);
      const unsigned vw3 = (lane >> 4) * (SF * 2) + (((lane & 15) ^ (((lane >> 4) << 2) | 2)) << 4);
      const char* xb = (const char*)p.x + (size_t)(mt * 128 + hw * 32) * 1024;
      const char* w1b = (const char*)p.w1 + (size_t)(hw * 16) * (SF * 2) + ft * 256;
      const char* w3b = (const char*)p.w3 + (size_t)(hw * 16) * (SF * 2) + ft * 256;
      auto issue = [&](int it) {
        const unsigned d = lb + (it % 3) * STG + hw * 4096;
        const int st = (it + rot) & 7;
#pragma unroll
        for (int j = 0; j < 4; ++j) {
          glds16(w1b + (size_t)(st * 64 + j * 4) * (SF * 2), vw1, d + 16384 + j * 1024);
          glds16(w3b + (size_t)(st * 64 + j * 4) * (SF * 2), vw3, d + 32768 + j * 1024);
          glds16(xb + j * 8192 + st * 128, (j & 1) ? vx1 : vx0, d + j * 1024);
        }
      };
    if (wv >= 4) {
      issue(0);
      issue(1);
#pragma unroll
      for (int j = 0; j < 2; ++j)
        glds16((const char*)p.w2 + (size_t)((un >> 1) * 16 + hw * 4 + j * 2) * 1024 + (un & 1) * 512,
               (lane >> 5) * 1024 + (((lane & 31) ^ ((2 * j + (lane >> 5)) << 2)) << 4), lb + L1_W2 + hw * 2048 + j * 1024);
      issue(2);
      bf16x8 ut[4];
#pragma unroll
      for (int i = 0; i < 8; ++i) {
        if (i == 0) VMW(26); else if (i == 1) VMW(14); else if (i < 7) VMW(12); else VMW(2);
        bar();
        if (i >= 1 && i < 6) issue(i + 2);
        if (ti == i) {
#pragma unroll
          for (int j = 0; j < 4; ++j) {
            const int r0 = j * 32 + g * 8 + q;
            const unsigned a0 = lb + (i % 3) * STG + r0 * 128 + (((2 * hw + (pp >> 1)) ^ ((r0 >> 1) & 7)) << 4) + (pp & 1) * 8;
            const unsigned a1 = lb + (i % 3) * STG + (r0 + 4) * 128 + (((2 * hw + (pp >> 1)) ^ (((r0 + 4) >> 1) & 7)) << 4) + (pp & 1) * 8;
            ut[j] = trfrag(a0, a1);
          }
        }
        if (i == 6) {
#pragma unroll
          for (int j = 0; j < 2; ++j) {
            const int db = hw * 2 + j;
            const unsigned a = lb + L1_W2 + (8 * (g >> 1) + q) * 512 + ((db ^ q) << 6) + (g & 1) * 32 + pp * 8;
            st16(p.ws + OFF_W2T + ((size_t)(((un & 1) * 8 + db) * 112 + (un >> 1)) << 10) + lane * 16, trfrag(a, a + 2048));
          }
        }
      }
      if (ft < 8) {
#pragma unroll
        for (int j = 0; j < 4; ++j)
          st16(p.ws + OFF_UT + ((size_t)(ft * 64 + hw * 16 + (lane & 15)) * SL + mt * 128 + j * 32 + g * 8) * 2, ut[j]);
      }
    } else {
      const int wm = wv & 1, wn = wv >> 1, r = lane & 31, h = lane >> 5;
      unsigned xa[4], wa[4];
#pragma unroll
      for (int s = 0; s < 4; ++s) xa[s] = lb + (wm * 64 + r) * 128 + (((2 * s + h) ^ ((r >> 1) & 7)) << 4);
#pragma unroll
      for (int c = 0; c < 4; ++c)
        wa[c] = lb + 16384 + (g & 1) * 16384 + (8 * (g >> 1) + q) * 256 + (((4 * wn + c) ^ ((q << 1) | (g & 1))) << 5) +
                ((((pp & 1) << 1) | (pp >> 1)) << 3);
      f32x16 acc[2][4];
#pragma unroll
      for (int t = 0; t < 2; ++t)
#pragma unroll
        for (int c = 0; c < 4; ++c)
#pragma unroll
          for (int e = 0; e < 16; ++e) acc[t][c][e] = 0.f;
      bf16x8 xf[2][2], wf[2][4];
      auto ld = [&](int gs) {
        const unsigned so = ((gs >> 2) % 3) * STG;
        const int s = gs & 3, u = gs & 1;
        wf[u][0] = trfrag(wa[0] + so + s * 4096, wa[0] + so + s * 4096 + 1024);
        xf[u][0] = __builtin_bit_cast(bf16x8, lds128(xa[s] + so));
        xf[u][1] = __builtin_bit_cast(bf16x8, lds128(xa[s] + so + 4096));
#pragma unroll
        for (int c = 1; c < 4; ++c) wf[u][c] = trfrag(wa[c] + so + s * 4096, wa[c] + so + s * 4096 + 1024);
      };
      bar();
      ST(0, 1);
      ld(0);
#pragma unroll
      for (int gs = 0; gs < 32; ++gs) {
        SB0;
        if ((gs & 3) == 3 && gs < 31) { bar(); ST(0, 2 + (gs >> 2)); SB0; }
        if (gs < 31) ld(gs + 1);
#pragma unroll
        for (int c = 0; c < 4; ++c) {
          acc[0][c] = mfma32(wf[gs & 1][c], xf[gs & 1][0], acc[0][c]);
          acc[1][c] = mfma32(wf[gs & 1][c], xf[gs & 1][1], acc[1][c]);
        }
#pragma unroll
        for (int j = 0; j < 5; ++j) { SGB(0x8, 1); SGB(0x100, 2); }
        SGB(0x8, 3);
      }
      SB0;
      ST(0, 9);
#pragma unroll
      for (int t = 0; t < 2; ++t)
#pragma unroll
        for (int c = 0; c < 4; ++c) {
          u32x4 o;
#pragma unroll
          for (int e = 0; e < 4; ++e) {
            const f32x2 gt = {acc[t][c][2 * e], acc[t][c][2 * e + 1]}, up = {acc[t][c][8 + 2 * e], acc[t][c][9 + 2 * e]};
            const f32x2 z = gt * -1.4426950408889634f;
            f32x2 ex = {__builtin_amdgcn_exp2f(z.x), __builtin_amdgcn_exp2f(z.y)};
            ex = ex + 1.f;
            const f32x2 rc = {__builtin_amdgcn_rcpf(ex.x), __builtin_amdgcn_rcpf(ex.y)};
            const f32x2 hv = (gt * rc) * up;
            o[e] = pk2(hv.x, hv.y);
          }
          st16(p.ws + OFF_HT + ((size_t)((mt * 4 + wm * 2 + t) * 112 + ft * 8 + wn * 4 + c) << 10) + lane * 16, __builtin_bit_cast(bf16x8, o));
        }
    }
  } else {
    const int sp = xcd * 4 + (jj - 28);
    u32x4 uv[8];
#pragma unroll
    for (int i = 0; i < 8; ++i) uv[i] = *(const u32x4*)(p.x + (size_t)(sp * 64 + wv * 8 + i) * SD + lane * 8);
    const u32x4 gw = *(const u32x4*)(p.gw + lane * 8);
    const u32x4 ones = {~0u, ~0u, ~0u, ~0u};
#pragma unroll
    for (int i = 0; i < 8; ++i) st16(p.ws + OFF_AT + (size_t)sp * 65536 + i * 8192 + tid * 16, __builtin_bit_cast(bf16x8, ones));
    float gsel = 0.f;
#pragma unroll
    for (int i = 0; i < 8; ++i) {
      float s = 0.f;
#pragma unroll
      for (int e = 0; e < 4; ++e) s += bf2f(uv[i][e]) * bf2f(gw[e]) + bfhi(uv[i][e]) * bfhi(gw[e]);
#pragma unroll
      for (int o = 32; o > 0; o >>= 1) s += __shfl_xor(s, o);
      if (lane == i) gsel = s;
    }
    if (lane < 8) ((float*)(p.ws + OFF_G))[sp * 64 + wv * 8 + lane] = rbf(1.f / (1.f + expf(-rbf(gsel))));
  }
  ST(0, 15);
}

constexpr unsigned KCS = 4160, L2_UI = 2 * 8 * KCS, UIS = 4608, L2_PX = L2_UI + 2 * UIS, L2_RD = L2_PX + 16384, ATS = 144;
constexpr unsigned RS = 272, L2_FL = L2_RD + 256 * RS, L2_TOTAL = L2_FL + 64;
static_assert(L2_TOTAL <= 163840, "");
#define KPRIO 2
#define CPRIO 0
#define PF 9

DI void pair_sync(unsigned a, unsigned target, int lane) {
  lgkm0();
  LAS unsigned* f = (LAS unsigned*)(uintptr_t)a;
  if (lane == 0) __hip_atomic_fetch_add(f, 1u, __ATOMIC_RELAXED, __HIP_MEMORY_SCOPE_WORKGROUP);
  while (__hip_atomic_load(f, __ATOMIC_RELAXED, __HIP_MEMORY_SCOPE_WORKGROUP) < target) __builtin_amdgcn_s_sleep(1);
  asm volatile("" ::: "memory");
}

__global__ __launch_bounds__(512, 2) void k2(P2 p) {
  extern __shared__ __attribute__((aligned(16))) char smem[];
  const unsigned lb = (unsigned)(uintptr_t)(LAS char*)smem;
  const int b = blockIdx.x, xcd = b & 7, jj = b >> 3, tid = threadIdx.x, lane = tid & 63;
  const int wv = __builtin_amdgcn_readfirstlane(tid >> 6);
  const int mt = (xcd >> 1) * 8 + (jj & 7), nt = (xcd & 1) * 4 + (jj >> 3);
  const int r = lane & 31, h = lane >> 5;
  const int ftok = 16 * (wv & 3) + (lane & 15), fo = 4 * (wv >> 2) + (lane >> 4);
  STDECL;
  ST(1, 0);
  if (tid < 16) *(LAS unsigned*)(uintptr_t)(lb + L2_FL + tid * 4) = 0u;
  const float gate = ((const float*)(p.ws + OFF_G))[mt * 64 + ftok];
  if (wv < 4) {
    const char* wa = p.ws + OFF_W2T + ((size_t)(2 * nt * 112 + 28 * wv) << 10);
    const char* hb = p.ws + OFF_HT + ((size_t)(2 * mt * 112 + 28 * wv) << 10);
#define ROT2 0
    int kq = (ROT2) % 28;
    bf16x8 A[PF][2], B[PF][2];
    auto ldg = [&](int u) {
      const unsigned o = kq * 1024 + lane * 16;
      B[u][0] = *(const bf16x8*)(hb + o); A[u][0] = *(const bf16x8*)(wa + o);
      B[u][1] = *(const bf16x8*)(hb + 112 * 1024 + o); A[u][1] = *(const bf16x8*)(wa + 112 * 1024 + o);
      kq = kq == 27 ? 0 : kq + 1;
    };
    bar();
#pragma unroll
    for (int s = 0; s < PF; ++s) ldg(s);
    SB0;
    f32x16 acc[2][2];
#pragma unroll
    for (int a = 0; a < 2; ++a)
#pragma unroll
      for (int t = 0; t < 2; ++t)
#pragma unroll
        for (int e = 0; e < 16; ++e) acc[a][t][e] = 0.f;
#define NS 28
#pragma unroll
    for (int s = 0; s < NS; ++s) {
      const int u = s % PF;
      acc[0][0] = mfma32(A[u][0], B[u][0], acc[0][0]);
      acc[0][1] = mfma32(A[u][0], B[u][1], acc[0][1]);
      acc[1][0] = mfma32(A[u][1], B[u][0], acc[1][0]);
      acc[1][1] = mfma32(A[u][1], B[u][1], acc[1][1]);
      if (s + PF < NS) ldg(u);
      SB0;
    }
    ST(1, 1);
#pragma unroll
    for (int a = 0; a < 2; ++a)
#pragma unroll
      for (int t = 0; t < 2; ++t)
#pragma unroll
        for (int q = 0; q < 4; ++q) {
          f32x4 v = {acc[a][t][4 * q], acc[a][t][4 * q + 1], acc[a][t][4 * q + 2], acc[a][t][4 * q + 3]};
          sts128(lb + L2_RD + (wv * 64 + 32 * t + r) * RS + (32 * a + 8 * q + 4 * h) * 4, __builtin_bit_cast(u32x4, v));
        }
  } else {
    const int cw = wv - 4, cl = cw >> 1, half = cw & 1, ch = 2 * b + cl;
    const u16* ut = (const u16*)(p.ws + OFF_UT) + (size_t)ch * SL;
    const unsigned kc = lb + cl * 8 * KCS, ui = lb + L2_UI + cl * UIS, ktb = lb + L2_PX + cl * 8192;
    const int po = ch * SN + half * 64 + lane;
    const unsigned pq[7] = {p.are[po], p.aim[po], p.bre[po], p.bim[po], p.cre[po], p.cim[po], p.ldt[ch]};
    u32x4 uu[2];
#pragma unroll
    for (int rd = 0; rd < 2; ++rd) uu[rd] = *(const u32x4*)(ut + 8 * (rd * 128 + half * 64 + lane));
    const float dsk = bf2f(p.dskip[ch]);
    bar();
    __builtin_amdgcn_s_setprio(KPRIO);
    kgen_table(pq, lane, kc + half * 16384);
    ST(1, 6);
    kgen_mma_put(lane, kc + half * 16384, ktb + half * 4);
    if (lane == 0) *(LAS float*)(uintptr_t)(ktb + 720 * 8 + half * 4) = 0.f;
    const u32x4 z4 = {0u, 0u, 0u, 0u};
    if (half && lane < 32) sts128(ui + lane * 16, z4);
#pragma unroll
    for (int rd = 0; rd < 2; ++rd) sts128(ui + 512 + (rd * 128 + half * 64 + lane) * 16, uu[rd]);
    pair_sync(lb + L2_FL + cl * 4, 2u, lane);
    ST(1, 7);
    {
      const int t = half * 64 + lane, l0 = 2047 - 16 * t;
      const float lf = (float)l0;
      f32x2 v[24];
#pragma unroll
      for (int e = 0; e < 24; e += 2) {
        const unsigned w = pk2(lf - (float)e, lf - (float)(e + 1));
        const int lb8 = l0 - (e & ~7);
        const int c0 = lb8 < 128 ? (lb8 < 0 ? 720 : l0 - e) : (int)(w & 0xffffu) - (133 << 7);
        const int c1 = lb8 < 128 ? (lb8 < 0 ? 720 : l0 - e - 1) : (int)(w >> 16) - (133 << 7);
        v[e] = *(const LAS f32x2*)(uintptr_t)(ktb + c0 * 8);
        v[e + 1] = *(const LAS f32x2*)(uintptr_t)(ktb + c1 * 8);
      }
      unsigned w[12], o[11];
#pragma unroll
      for (int e = 0; e < 12; ++e) w[e] = pk2(v[2 * e].x + v[2 * e].y, v[2 * e + 1].x + v[2 * e + 1].y);
#pragma unroll
      for (int e = 0; e < 11; ++e) o[e] = __builtin_amdgcn_alignbit(w[e + 1], w[e], 16);
#pragma unroll
      for (int c = 0; c < 4; ++c)
#pragma unroll
        for (int d = 0; d < 2; ++d) {
          const int x = c + 4 * d;
          sts128(kc + (2 * c) * KCS + t * 32 + d * 16, u32x4{w[x], w[x + 1], w[x + 2], w[x + 3]});
          sts128(kc + (2 * c + 1) * KCS + t * 32 + d * 16, u32x4{o[x], o[x + 1], o[x + 2], o[x + 3]});
        }
      if (!half && lane < 32) sts128(kc + (lane >> 2) * KCS + (256 + (lane & 3)) * 16, z4);
    }
    pair_sync(lb + L2_FL + cl * 4, 4u, lane);
    __builtin_amdgcn_s_setprio(CPRIO);
    ST(1, 1);
    const int m = lane & 15, q = lane >> 4;
    const unsigned ka = kc + ((7 - m) & 7) * KCS + (2024 - 8 * (m >> 3) + 8 * q - 32 * (56 + 4 * half + 3)) * 2;
    const unsigned ua = ui + 512 + (16 * m + 8 * q - 16 - 32 * (4 * half + 3)) * 2;
    f32x4 ac[8];
#pragma unroll
    for (int e = 0; e < 8; ++e) ac[e] = f32x4{0.f, 0.f, 0.f, 0.f};
    bf16x8 fa[2][8], fb[2][8];
    auto ldc = [&](int it) {
#pragma unroll
      for (int i = 0; i < 8; ++i) {
        fa[it & 1][i] = __builtin_bit_cast(bf16x8, lds128(ka + it * 64 + (7 - i) * 512));
        fb[it & 1][i] = __builtin_bit_cast(bf16x8, lds128(ua + it * 64 + i * 512));
      }
    };
    ldc(0);
#pragma unroll
    for (int it = 0; it < 4; ++it) {
      SB0;
      if (it < 3) ldc(it + 1);
#pragma unroll
      for (int d = 0; d < 8; ++d)
#pragma unroll
        for (int i = 0; i <= d; ++i) {
          if (i + d < 8) ac[i + d] = mfma16(fa[it & 1][i], fb[it & 1][d], ac[i + d]);
          if (i < d && i + d < 8) ac[i + d] = mfma16(fa[it & 1][d], fb[it & 1][i], ac[i + d]);
        }
#pragma unroll
      for (int j = 0; j < 16; ++j) { SGB(0x8, 2); SGB(0x100, 1); }
      SGB(0x8, 4);
    }
    SB0;
    if (half) {
#pragma unroll
      for (int j = 0; j < 4; ++j) { const f32x4 t = ac[j]; ac[j] = ac[4 + j]; ac[4 + j] = t; asm volatile("" : "+v"(ac[j]), "+v"(ac[4 + j])); }
    }
#pragma unroll
    for (int j = 0; j < 4; ++j) sts128(lb + L2_PX + cw * 4096 + j * 1024 + lane * 16, __builtin_bit_cast(u32x4, ac[4 + j]));
    pair_sync(lb + L2_FL + cl * 4, 6u, lane);
    ST(1, 2);
    __builtin_amdgcn_s_setprio(3);
    const __amdgpu_buffer_rsrc_t ars = __builtin_amdgcn_make_buffer_rsrc(p.ws + OFF_AT, (short)0, SL * SD * 2, 0x00020000);
#pragma unroll
    for (int j = 0; j < 4; ++j) {
      const int l = 256 * (4 * half + j) + 16 * m + 4 * q;
      const u32x2 uq = *(const LAS u32x2*)(uintptr_t)(ui + 512 + l * 2);
      const f32x4 v = __builtin_bit_cast(f32x4, lds128(lb + L2_PX + (cw ^ 1) * 4096 + j * 1024 + lane * 16)) + ac[j];
      u32x2 o;
#pragma unroll
      for (int e = 0; e < 2; ++e) {
        const unsigned y = pk2(v[2 * e], v[2 * e + 1]), sk = pk2(bf2f(uq[e]) * dsk, bfhi(uq[e]) * dsk);
        const unsigned w = pk2(bf2f(y) + bf2f(sk), bfhi(y) + bfhi(sk));
        o[e] = w == ~0u ? 0x7fc07fc0u : w;
      }
      __builtin_amdgcn_raw_buffer_store_b64(o, ars, (ch * SL + l) * 2, 0, 16);
    }
    ST(1, 3);
    u32x4 av[2];
#pragma unroll 1
    for (int spin = 0; spin < (1 << 20); ++spin) {
      bool bad = false;
#pragma unroll
      for (int i = 0; i < 2; ++i) {
        av[i] = __builtin_amdgcn_raw_buffer_load_b128(ars, ((nt * 64 + 16 * cw + 8 * i + (lane >> 3)) * SL + mt * 64 + (lane & 7) * 8) * 2, 0, 16);
        bad |= av[i].x == ~0u || av[i].y == ~0u || av[i].z == ~0u || av[i].w == ~0u;
      }
      if (!__any(bad)) break;
      __builtin_amdgcn_s_sleep(8);
      asm volatile("" ::: "memory");
    }
#pragma unroll
    for (int i = 0; i < 2; ++i) sts128(lb + L2_PX + (cw ^ 1) * 4096 + (8 * i + (lane >> 3)) * ATS + (lane & 7) * 16, av[i]);
    ST(1, 5);
    __builtin_amdgcn_s_setprio(0);
  }
  bar();
  ST(1, 14);
  {
    const unsigned aa = lb + L2_PX + ((fo >> 1) ^ 1) * 4096 + (8 * (fo & 1) + ((lane & 15) >> 2)) * ATS + (16 * (wv & 3) + (lane & 3) * 4) * 2;
    const u32x4 av = __builtin_bit_cast(u32x4, trfrag(aa, aa + 4 * ATS));
    f32x4 s0 = {0.f, 0.f, 0.f, 0.f}, s1 = s0;
#pragma unroll
    for (int w = 0; w < 4; ++w) {
      s0 += __builtin_bit_cast(f32x4, lds128(lb + L2_RD + (w * 64 + ftok) * RS + fo * 32));
      s1 += __builtin_bit_cast(f32x4, lds128(lb + L2_RD + (w * 64 + ftok) * RS + fo * 32 + 16));
    }
    const float og = rbf(1.f - gate);
    u32x4 o;
#pragma unroll
    for (int e = 0; e < 4; ++e) {
      const float b0 = e < 2 ? s0[2 * e] : s1[2 * e - 4], b1 = e < 2 ? s0[2 * e + 1] : s1[2 * e - 3];
      o[e] = pk2(rbf(gate * bf2f(av[e])) + rbf(og * rbf(b0)), rbf(gate * bfhi(av[e])) + rbf(og * rbf(b1)));
    }
    *(u32x4*)(p.out + (size_t)(mt * 64 + ftok) * SD + nt * 64 + fo * 8) = o;
  }
  ST(1, 15);
}

extern "C" void kernel_launch(void* const* d_in, const int* in_sizes, int n_in, void* d_out, int out_size, void* d_ws,
                              size_t ws_size, hipStream_t stream) {
  P1 a;
  a.x = (const u16*)d_in[0]; a.w1 = (const u16*)d_in[10]; a.w3 = (const u16*)d_in[11]; a.w2 = (const u16*)d_in[12];
  a.gw = (const u16*)d_in[13]; a.ws = (char*)d_ws;
  P2 c;
  c.are = (const u16*)d_in[1]; c.aim = (const u16*)d_in[2]; c.bre = (const u16*)d_in[3]; c.bim = (const u16*)d_in[4];
  c.cre = (const u16*)d_in[5]; c.cim = (const u16*)d_in[6]; c.ldt = (const u16*)d_in[7]; c.dskip = (const u16*)d_in[8];
  c.out = (u16*)d_out; c.ws = (char*)d_ws;
  (void)hipFuncSetAttribute((const void*)k1, hipFuncAttributeMaxDynamicSharedMemorySize, L1_TOTAL);
  (void)hipFuncSetAttribute((const void*)k2, hipFuncAttributeMaxDynamicSharedMemorySize, L2_TOTAL);
  k1<<<256, 512, L1_TOTAL, stream>>>(a);
  k2<<<256, 512, L2_TOTAL, stream>>>(c);
}
```

```cpp
#include <hip/hip_runtime.h>
#include <stdint.h>

typedef unsigned short u16;
typedef __bf16 bf16x8 __attribute__((ext_vector_type(8)));
typedef __bf16 bf16x2 __attribute__((ext_vector_type(2)));
typedef short s16x4 __attribute__((ext_vector_type(4)));
typedef float f32x2 __attribute__((ext_vector_type(2)));
typedef float f32x4 __attribute__((ext_vector_type(4)));
typedef float f32x16 __attribute__((ext_vector_type(16)));
typedef unsigned u32x2 __attribute__((ext_vector_type(2)));
typedef unsigned u32x4 __attribute__((ext_vector_type(4)));
#define LAS __attribute__((address_space(3)))
#define GAS __attribute__((address_space(1)))
#define DI __device__ __forceinline__

constexpr int SL = 2048, SD = 512, SN = 128, SF = 1792;
constexpr size_t OFF_HT = 0;
constexpr size_t OFF_W2T = OFF_HT + (size_t)SL * SF * 2;
constexpr size_t OFF_UT = OFF_W2T + (size_t)SF * SD * 2;
constexpr size_t OFF_AT = OFF_UT + (size_t)SL * SD * 2;
constexpr size_t OFF_G = OFF_AT + (size_t)SL * SD * 2;

#define ST(K, J)
#define STDECL
DI float bf2f(unsigned v) { return __builtin_bit_cast(float, v << 16); }
DI float bfhi(unsigned v) { return __builtin_bit_cast(float, v & 0xffff0000u); }
DI unsigned pk2(float a, float b) { f32x2 v = {a, b}; return __builtin_bit_cast(unsigned, __builtin_convertvector(v, bf16x2)); }
DI float rbf(float a) { return bf2f(pk2(a, 0.f) & 0xffffu); }
DI void lgkm0() { asm volatile("s_waitcnt lgkmcnt(0)" ::: "memory"); }
DI void bar() { asm volatile("s_waitcnt lgkmcnt(0)\n\ts_barrier" ::: "memory"); }
DI void glds16(const void* sbase, unsigned voff, unsigned lds) {
  asm volatile("s_mov_b32 m0, %0\n\ts_nop 0\n\tglobal_load_lds_dwordx4 %1, %2" ::"s"(lds), "v"(voff), "s"(sbase) : "memory");
}
DI u32x4 lds128(unsigned a) { return *(const LAS u32x4*)(uintptr_t)a; }
DI void sts128(unsigned a, u32x4 v) { *(LAS u32x4*)(uintptr_t)a = v; }
DI bf16x8 trfrag(unsigned a0, unsigned a1) {
  s16x4 lo = __builtin_amdgcn_ds_read_tr16_b64_v4i16((LAS s16x4*)(uintptr_t)a0);
  s16x4 hi = __builtin_amdgcn_ds_read_tr16_b64_v4i16((LAS s16x4*)(uintptr_t)a1);
  u32x2 l = __builtin_bit_cast(u32x2, lo), h = __builtin_bit_cast(u32x2, hi);
  u32x4 r = {l.x, l.y, h.x, h.y};
  return __builtin_bit_cast(bf16x8, r);
}
#define WT 1
DI void st16(void* a, bf16x8 v) {
  asm volatile("global_store_dwordx4 %0, %1, off sc0 sc1\n\ts_nop 1" ::"v"(a), "v"(v) : "memory");
}
DI f32x16 mfma32(bf16x8 a, bf16x8 b, f32x16 c) { return __builtin_amdgcn_mfma_f32_32x32x16_bf16(a, b, c, 0, 0, 0); }
DI f32x4 mfma16(bf16x8 a, bf16x8 b, f32x4 c) { return __builtin_amdgcn_mfma_f32_16x16x32_bf16(a, b, c, 0, 0, 0); }

struct P1 {
  const u16 *x, *w1, *w3, *w2, *gw;
  char* ws;
};
struct P2 {
  const u16 *are, *aim, *bre, *bim, *cre, *cim, *ldt, *dskip;
  u16* out;
  char* ws;
};

constexpr unsigned STG = 49152, L1_W2 = 3 * STG, L1_TOTAL = L1_W2 + 8192;

#define SB0 __builtin_amdgcn_sched_barrier(0)
struct cf { float x, y; };
DI cf cmul(cf a, cf b) {
  cf r;
  asm("v_mul_f32 %0, %2, %4\n\tv_mul_f32 %1, %2, %5\n\tv_fma_f32 %0, -%3, %5, %0\n\tv_fma_f32 %1, %3, %4, %1"
      : "=&v"(r.x), "=&v"(r.y) : "v"(a.x), "v"(a.y), "v"(b.x), "v"(b.y));
  return r;
}
DI cf cexp(float a, float rev) {
  rev = __builtin_amdgcn_fractf(rev);
  const float m = __builtin_amdgcn_exp2f(a);
  cf r = {__builtin_amdgcn_cosf(rev) * m, __builtin_amdgcn_sinf(rev) * m};
  return r;
}
DI void kgen_table(const unsigned (&q)[7], int lane, unsigned tb) {
  const float dt = rbf(expf(bf2f(q[6])));
  const float dbr = dt * bf2f(q[2]), dbi = dt * bf2f(q[3]), cr = bf2f(q[4]), ci = bf2f(q[5]);
  const float a = (dt * bf2f(q[0])) * 1.4426950408889634f, rev = (dt * bf2f(q[1])) * 0.15915494309189535f;
  cf e[16], bq[16], gq[9];
  e[0] = cf{1.f, 0.f}; e[1] = cexp(a, rev);
  const cf c16 = cexp(16.f * a, -16.f * rev);
  gq[1] = cexp(256.f * a, 256.f * rev);
  bq[0] = cf{cr * dbr - ci * dbi, -(cr * dbi + ci * dbr)};
  const cf c32 = cmul(c16, c16), c64 = cmul(c32, c32);
  bq[1] = cmul(bq[0], c16); bq[2] = cmul(bq[0], c32); bq[3] = cmul(bq[1], c32);
#pragma unroll
  for (int j = 2; j < 16; ++j) e[j] = cmul(e[j >> 1], e[j - (j >> 1)]);
#pragma unroll
  for (int j = 2; j < 9; ++j) gq[j] = cmul(gq[j >> 1], gq[j - (j >> 1)]);
#pragma unroll
  for (int j = 4; j < 16; ++j) bq[j] = cmul(bq[j - 4], c64);
#pragma unroll
  for (int r = 0; r < 16; ++r) {
    LAS unsigned* w = (LAS unsigned*)(uintptr_t)(((tb + lane * 4) ^ (r << 4)) + r * 256);
    const cf v1 = r < 8 ? (r ? cmul(gq[1], e[2 * r]) : gq[1]) : ((r & 3) ? cmul(gq[r >> 2], e[4 * (r & 3)]) : gq[r >> 2]);
    w[0] = pk2(bq[r].x, bq[r].y);
    w[1024] = pk2(e[r].x, e[r].y);
    w[2048] = pk2(v1.x, v1.y);
    if (r <= 8) { const cf v2 = (r & 1) ? cmul(gq[4 + (r >> 1)], e[8]) : gq[4 + (r >> 1)]; w[3072] = pk2(v2.x, v2.y); }
    else w[3072] = 0u;
  }
}
DI void kgen_mma_put(int lane, unsigned tb, unsigned ktb) {
  const int j0 = (lane >> 4) * 4, r = lane & 15;
  bf16x8 f[4][4];
  f32x4 acc[3];
#pragma unroll
  for (int ks = 0; ks < 4; ++ks)
#pragma unroll
    for (int t = 0; t < 4; ++t) f[ks][t] = __builtin_bit_cast(bf16x8, lds128(tb + t * 4096 + r * 256 + (((ks * 4 + (lane >> 4)) ^ r) << 4)));
  SB0;
#pragma unroll
  for (int t = 0; t < 3; ++t) acc[t] = f32x4{0.f, 0.f, 0.f, 0.f};
#pragma unroll
  for (int ks = 0; ks < 4; ++ks)
#pragma unroll
    for (int t = 0; t < 3; ++t) acc[t] = mfma16(f[ks][0], f[ks][t + 1], acc[t]);
  LAS float* kt = (LAS float*)(uintptr_t)ktb;
  const int c1 = r < 8 ? 256 + r + 8 * j0 : 256 + 64 * (r >> 2) + (r & 3) + 4 * j0, s1 = r < 8 ? 8 : 4;
  const int c2 = (r <= 8 ? 512 + 32 * (r >> 1) + (r & 1) : 680) + 2 * j0;
#pragma unroll
  for (int e = 0; e < 4; ++e) {
    kt[(r + 16 * (j0 + e)) * 2] = acc[0][e];
    kt[(c1 + s1 * e) * 2] = acc[1][e];
    kt[(c2 + 2 * e) * 2] = acc[2][e];
  }
}

#define SGB(m, n) __builtin_amdgcn_sched_group_barrier(m, n, 0)
#define VMW(n) asm volatile("s_waitcnt vmcnt(" #n ")" ::: "memory")
#define MAP 0

__global__ __launch_bounds__(512, 2) void k1(P1 p) {
  extern __shared__ __attribute__((aligned(16))) char smem[];
  const unsigned lb = (unsigned)(uintptr_t)(LAS char*)smem;
  const int b = blockIdx.x, xcd = b & 7, jj = b >> 3, tid = threadIdx.x, lane = tid & 63;
  const int wv = __builtin_amdgcn_readfirstlane(tid >> 6);
  const int g = lane >> 4, q = (lane & 15) >> 2, pp = lane & 3;
  STDECL;
  ST(0, 0);
  if (jj < 28) {
    const int mt = (xcd >> 1) * 4 + (jj & 3), ft = (xcd & 1) * 7 + (jj >> 2);
#define ROT1 0
    const int rot = ROT1, ti = ft < 8 ? (ft - rot) & 7 : -9;
      const int hw = wv & 3, un = mt * 14 + ft;
      const unsigned vx0 = (lane >> 3) * 1024 + (((lane & 7) ^ ((lane >> 4) & 7)) << 4);
      const unsigned vx1 = (lane >> 3) * 1024 + (((lane & 7) ^ ((4 + (lane >> 4)) & 7)) << 4);
      const unsigned vw1 = (lane >> 4) * (SF * 2) + (((lane & 15) ^ ((lane >> 4) << 2)) << 4);
      const unsigned vw3 = (lane >> 4) * (SF * 2) + (((lane & 15) ^ (((lane >> 4) << 2) | 2)) << 4);
      const char* xb = (const char*)p.x + (size_t)(mt * 128 + hw * 32) * 1024;
      const char* w1b = (const char*)p.w1 + (size_t)(hw * 16) * (SF * 2) + ft * 256;
      const char* w3b = (const char*)p.w3 + (size_t)(hw * 16) * (SF * 2) + ft * 256;
      auto issue = [&](int it) {
        const unsigned d = lb + (it % 3) * STG + hw * 4096;
        const int st = (it + rot) & 7;
#pragma unroll
        for (int j = 0; j < 4; ++j) {
          glds16(w1b + (size_t)(st * 64 + j * 4) * (SF * 2), vw1, d + 16384 + j * 1024);
          glds16(w3b + (size_t)(st * 64 + j * 4) * (SF * 2), vw3, d + 32768 + j * 1024);
          glds16(xb + j * 8192 + st * 128, (j & 1) ? vx1 : vx0, d + j * 1024);
        }
      };
    if (wv >= 4) {
      issue(0);
      issue(1);
#pragma unroll
      for (int j = 0; j < 2; ++j)
        glds16((const char*)p.w2 + (size_t)((un >> 1) * 16 + hw * 4 + j * 2) * 1024 + (un & 1) * 512,
               (lane >> 5) * 1024 + (((lane & 31) ^ ((2 * j + (lane >> 5)) << 2)) << 4), lb + L1_W2 + hw * 2048 + j * 1024);
      issue(2);
      bf16x8 ut[4];
#pragma unroll
      for (int i = 0; i < 8; ++i) {
        if (i == 0) VMW(26); else if (i == 1) VMW(14); else if (i < 7) VMW(12); else VMW(2);
        bar();
        if (i >= 1 && i < 6) issue(i + 2);
        if (ti == i) {
#pragma unroll
          for (int j = 0; j < 4; ++j) {
            const int r0 = j * 32 + g * 8 + q;
            const unsigned a0 = lb + (i % 3) * STG + r0 * 128 + (((2 * hw + (pp >> 1)) ^ ((r0 >> 1) & 7)) << 4) + (pp & 1) * 8;
            const unsigned a1 = lb + (i % 3) * STG + (r0 + 4) * 128 + (((2 * hw + (pp >> 1)) ^ (((r0 + 4) >> 1) & 7)) << 4) + (pp & 1) * 8;
            ut[j] = trfrag(a0, a1);
          }
        }
        if (i == 6) {
#pragma unroll
          for (int j = 0; j < 2; ++j) {
            const int db = hw * 2 + j;
            const unsigned a = lb + L1_W2 + (8 * (g >> 1) + q) * 512 + ((db ^ q) << 6) + (g & 1) * 32 + pp * 8;
            st16(p.ws + OFF_W2T + ((size_t)(((un & 1) * 8 + db) * 112 + (un >> 1)) << 10) + lane * 16, trfrag(a, a + 2048));
          }
        }
      }
      if (ft < 8) {
#pragma unroll
        for (int j = 0; j < 4; ++j)
          st16(p.ws + OFF_UT + ((size_t)(ft * 64 + hw * 16 + (lane & 15)) * SL + mt * 128 + j * 32 + g * 8) * 2, ut[j]);
      }
    } else {
      const int wm = wv & 1, wn = wv >> 1, r = lane & 31, h = lane >> 5;
      unsigned xa[4], wa[4];
#pragma unroll
      for (int s = 0; s < 4; ++s) xa[s] = lb + (wm * 64 + r) * 128 + (((2 * s + h) ^ ((r >> 1) & 7)) << 4);
#pragma unroll
      for (int c = 0; c < 4; ++c)
        wa[c] = lb + 16384 + (g & 1) * 16384 + (8 * (g >> 1) + q) * 256 + (((4 * wn + c) ^ ((q << 1) | (g & 1))) << 5) +
                ((((pp & 1) << 1) | (pp >> 1)) << 3);
      f32x16 acc[2][4];
#pragma unroll
      for (int t = 0; t < 2; ++t)
#pragma unroll
        for (int c = 0; c < 4; ++c)
#pragma unroll
          for (int e = 0; e < 16; ++e) acc[t][c][e] = 0.f;
      bf16x8 xf[2][2], wf[2][4];
      auto ld = [&](int gs) {
        const unsigned so = 0;
        const int s = gs & 3, u = gs & 1;
        wf[u][0] = trfrag(wa[0] + so + s * 4096, wa[0] + so + s * 4096 + 1024);
        xf[u][0] = __builtin_bit_cast(bf16x8, lds128(xa[s] + so));
        xf[u][1] = __builtin_bit_cast(bf16x8, lds128(xa[s] + so + 4096));
#pragma unroll
        for (int c = 1; c < 4; ++c) wf[u][c] = trfrag(wa[c] + so + s * 4096, wa[c] + so + s * 4096 + 1024);
      };
      bar();
      ST(0, 1);
      ld(0);
#pragma unroll 1
      for (int st = 0; st < 8; ++st)
#pragma unroll
      for (int gs = 0; gs < 4; ++gs) {
        SB0;
        if (gs == 3) {
          const unsigned d = (st == 2 || st == 5) ? 0u - 2 * STG : STG;
#pragma unroll
          for (int c = 0; c < 4; ++c) { xa[c] += d; wa[c] += d; }
          if (st < 7) bar();
          SB0;
        }
        ld((gs + 1) & 3);
#pragma unroll
        for (int c = 0; c < 4; ++c) {
          acc[0][c] = mfma32(wf[gs & 1][c], xf[gs & 1][0], acc[0][c]);
          acc[1][c] = mfma32(wf[gs & 1][c], xf[gs & 1][1], acc[1][c]);
        }
#pragma unroll
        for (int j = 0; j < 5; ++j) { SGB(0x8, 1); SGB(0x100, 2); }
        SGB(0x8, 3);
      }
      SB0;
      ST(0, 9);
#pragma unroll
      for (int t = 0; t < 2; ++t)
#pragma unroll
        for (int c = 0; c < 4; ++c) {
          u32x4 o;
#pragma unroll
          for (int e = 0; e < 4; ++e) {
            const f32x2 gt = {acc[t][c][2 * e], acc[t][c][2 * e + 1]}, up = {acc[t][c][8 + 2 * e], acc[t][c][9 + 2 * e]};
            const f32x2 z = gt * -1.4426950408889634f;
            f32x2 ex = {__builtin_amdgcn_exp2f(z.x), __builtin_amdgcn_exp2f(z.y)};
            ex = ex + 1.f;
            const f32x2 rc = {__builtin_amdgcn_rcpf(ex.x), __builtin_amdgcn_rcpf(ex.y)};
            const f32x2 hv = (gt * rc) * up;
            o[e] = pk2(hv.x, hv.y);
          }
          st16(p.ws + OFF_HT + ((size_t)((mt * 4 + wm * 2 + t) * 112 + ft * 8 + wn * 4 + c) << 10) + lane * 16, __builtin_bit_cast(bf16x8, o));
        }
    }
  } else {
    const int sp = xcd * 4 + (jj - 28);
    u32x4 uv[8];
#pragma unroll
    for (int i = 0; i < 8; ++i) uv[i] = *(const u32x4*)(p.x + (size_t)(sp * 64 + wv * 8 + i) * SD + lane * 8);
    const u32x4 gw = *(const u32x4*)(p.gw + lane * 8);
    const u32x4 ones = {~0u, ~0u, ~0u, ~0u};
#pragma unroll
    for (int i = 0; i < 8; ++i) st16(p.ws + OFF_AT + (size_t)sp * 65536 + i * 8192 + tid * 16, __builtin_bit_cast(bf16x8, ones));
    float gsel = 0.f;
#pragma unroll
    for (int i = 0; i < 8; ++i) {
      float s = 0.f;
#pragma unroll
      for (int e = 0; e < 4; ++e) s += bf2f(uv[i][e]) * bf2f(gw[e]) + bfhi(uv[i][e]) * bfhi(gw[e]);
#pragma unroll
      for (int o = 32; o > 0; o >>= 1) s += __shfl_xor(s, o);
      if (lane == i) gsel = s;
    }
    if (lane < 8) ((float*)(p.ws + OFF_G))[sp * 64 + wv * 8 + lane] = rbf(1.f / (1.f + expf(-rbf(gsel))));
  }
  ST(0, 15);
}

constexpr unsigned KCS = 4160, L2_UI = 2 * 8 * KCS, UIS = 4608, L2_PX = L2_UI + 2 * UIS, L2_RD = L2_PX + 16384, ATS = 144;
constexpr unsigned RS = 272, L2_FL = L2_RD + 256 * RS, L2_TOTAL = L2_FL + 64;
static_assert(L2_TOTAL <= 163840, "");
#define KPRIO 3
#define CPRIO 3
#define PF 9

DI void pair_sync(unsigned a, unsigned target, int lane) {
  lgkm0();
  LAS unsigned* f = (LAS unsigned*)(uintptr_t)a;
  if (lane == 0) __hip_atomic_fetch_add(f, 1u, __ATOMIC_RELAXED, __HIP_MEMORY_SCOPE_WORKGROUP);
  while (__hip_atomic_load(f, __ATOMIC_RELAXED, __HIP_MEMORY_SCOPE_WORKGROUP) < target) __builtin_amdgcn_s_sleep(1);
  asm volatile("" ::: "memory");
}

__global__ __launch_bounds__(512, 2) void k2(P2 p) {
  extern __shared__ __attribute__((aligned(16))) char smem[];
  const unsigned lb = (unsigned)(uintptr_t)(LAS char*)smem;
  const int b = blockIdx.x, xcd = b & 7, jj = b >> 3, tid = threadIdx.x, lane = tid & 63;
  const int wv = __builtin_amdgcn_readfirstlane(tid >> 6);
  const int mt = (xcd >> 1) * 8 + (jj & 7), nt = (xcd & 1) * 4 + (jj >> 3);
  const int r = lane & 31, h = lane >> 5;
  const int ftok = 16 * (wv & 3) + (lane & 15), fo = 4 * (wv >> 2) + (lane >> 4);
  STDECL;
  ST(1, 0);
  if (tid < 16) *(LAS unsigned*)(uintptr_t)(lb + L2_FL + tid * 4) = 0u;
  const float gate = ((const float*)(p.ws + OFF_G))[mt * 64 + ftok];
  if (wv < 4) {
    const char* wa = p.ws + OFF_W2T + ((size_t)(2 * nt * 112 + 28 * wv) << 10);
    const char* hb = p.ws + OFF_HT + ((size_t)(2 * mt * 112 + 28 * wv) << 10);
#define ROT2 0
    int kq = (ROT2) % 28;
    bf16x8 A[PF][2], B[PF][2];
    auto ldg = [&](int u) {
      const unsigned o = kq * 1024 + lane * 16;
      B[u][0] = *(const bf16x8*)(hb + o); A[u][0] = *(const bf16x8*)(wa + o);
      B[u][1] = *(const bf16x8*)(hb + 112 * 1024 + o); A[u][1] = *(const bf16x8*)(wa + 112 * 1024 + o);
      kq = kq == 27 ? 0 : kq + 1;
    };
    bar();
#pragma unroll
    for (int s = 0; s < PF; ++s) ldg(s);
    SB0;
    f32x16 acc[2][2];
#pragma unroll
    for (int a = 0; a < 2; ++a)
#pragma unroll
      for (int t = 0; t < 2; ++t)
#pragma unroll
        for (int e = 0; e < 16; ++e) acc[a][t][e] = 0.f;
#define NS 28
#pragma unroll
    for (int s = 0; s < NS; ++s) {
      const int u = s % PF;
      acc[0][0] = mfma32(A[u][0], B[u][0], acc[0][0]);
      acc[0][1] = mfma32(A[u][0], B[u][1], acc[0][1]);
      acc[1][0] = mfma32(A[u][1], B[u][0], acc[1][0]);
      acc[1][1] = mfma32(A[u][1], B[u][1], acc[1][1]);
      if (s + PF < NS) ldg(u);
      SB0;
    }
    ST(1, 1);
#pragma unroll
    for (int a = 0; a < 2; ++a)
#pragma unroll
      for (int t = 0; t < 2; ++t)
#pragma unroll
        for (int q = 0; q < 4; ++q) {
          f32x4 v = {acc[a][t][4 * q], acc[a][t][4 * q + 1], acc[a][t][4 * q + 2], acc[a][t][4 * q + 3]};
          sts128(lb + L2_RD + (wv * 64 + 32 * t + r) * RS + (32 * a + 8 * q + 4 * h) * 4, __builtin_bit_cast(u32x4, v));
        }
  } else {
    const int cw = wv - 4, cl = cw >> 1, half = cw & 1, ch = 2 * b + cl;
    const u16* ut = (const u16*)(p.ws + OFF_UT) + (size_t)ch * SL;
    const unsigned kc = lb + cl * 8 * KCS, ui = lb + L2_UI + cl * UIS, ktb = lb + L2_PX + cl * 8192;
    const int po = ch * SN + half * 64 + lane;
    const unsigned pq[7] = {p.are[po], p.aim[po], p.bre[po], p.bim[po], p.cre[po], p.cim[po], p.ldt[ch]};
    u32x4 uu[2];
#pragma unroll
    for (int rd = 0; rd < 2; ++rd) uu[rd] = *(const u32x4*)(ut + 8 * (rd * 128 + half * 64 + lane));
    const float dsk = bf2f(p.dskip[ch]);
    bar();
    __builtin_amdgcn_s_setprio(KPRIO);
    kgen_table(pq, lane, kc + half * 16384);
    ST(1, 6);
    kgen_mma_put(lane, kc + half * 16384, ktb + half * 4);
    if (lane == 0) *(LAS float*)(uintptr_t)(ktb + 720 * 8 + half * 4) = 0.f;
    const u32x4 z4 = {0u, 0u, 0u, 0u};
    if (half && lane < 32) sts128(ui + lane * 16, z4);
#pragma unroll
    for (int rd = 0; rd < 2; ++rd) sts128(ui + 512 + (rd * 128 + half * 64 + lane) * 16, uu[rd]);
    pair_sync(lb + L2_FL + cl * 4, 2u, lane);
    ST(1, 7);
    {
      const int t = half * 64 + lane, l0 = 2047 - 16 * t;
      const float lf = (float)l0;
      f32x2 v[24];
#pragma unroll
      for (int e = 0; e < 24; e += 2) {
        const unsigned w = pk2(lf - (float)e, lf - (float)(e + 1));
        const int lb8 = l0 - (e & ~7);
        const int c0 = lb8 < 128 ? (lb8 < 0 ? 720 : l0 - e) : (int)(w & 0xffffu) - (133 << 7);
        const int c1 = lb8 < 128 ? (lb8 < 0 ? 720 : l0 - e - 1) : (int)(w >> 16) - (133 << 7);
        v[e] = *(const LAS f32x2*)(uintptr_t)(ktb + c0 * 8);
        v[e + 1] = *(const LAS f32x2*)(uintptr_t)(ktb + c1 * 8);
      }
      unsigned w[12], o[11];
#pragma unroll
      for (int e = 0; e < 12; ++e) w[e] = pk2(v[2 * e].x + v[2 * e].y, v[2 * e + 1].x + v[2 * e + 1].y);
#pragma unroll
      for (int e = 0; e < 11; ++e) o[e] = __builtin_amdgcn_alignbit(w[e + 1], w[e], 16);
#pragma unroll
      for (int c = 0; c < 4; ++c)
#pragma unroll
        for (int d = 0; d < 2; ++d) {
          const int x = c + 4 * d;
          sts128(kc + (2 * c) * KCS + t * 32 + d * 16, u32x4{w[x], w[x + 1], w[x + 2], w[x + 3]});
          sts128(kc + (2 * c + 1) * KCS + t * 32 + d * 16, u32x4{o[x], o[x + 1], o[x + 2], o[x + 3]});
        }
      if (!half && lane < 32) sts128(kc + (lane >> 2) * KCS + (256 + (lane & 3)) * 16, z4);
    }
    pair_sync(lb + L2_FL + cl * 4, 4u, lane);
    __builtin_amdgcn_s_setprio(CPRIO);
    ST(1, 1);
    const int m = lane & 15, q = lane >> 4;
    const unsigned ka = kc + ((7 - m) & 7) * KCS + (2024 - 8 * (m >> 3) + 8 * q - 32 * (56 + 4 * half + 3)) * 2;
    const unsigned ua = ui + 512 + (16 * m + 8 * q - 16 - 32 * (4 * half + 3)) * 2;
    f32x4 ac[8];
#pragma unroll
    for (int e = 0; e < 8; ++e) ac[e] = f32x4{0.f, 0.f, 0.f, 0.f};
    bf16x8 fa[2][8], fb[2][8];
    auto ldc = [&](int it) {
#pragma unroll
      for (int i = 0; i < 8; ++i) {
        fa[it & 1][i] = __builtin_bit_cast(bf16x8, lds128(ka + it * 64 + (7 - i) * 512));
        fb[it & 1][i] = __builtin_bit_cast(bf16x8, lds128(ua + it * 64 + i * 512));
      }
    };
    ldc(0);
#pragma unroll
    for (int it = 0; it < 4; ++it) {
      SB0;
      if (it < 3) ldc(it + 1);
#pragma unroll
      for (int d = 0; d < 8; ++d)
#pragma unroll
        for (int i = 0; i <= d; ++i) {
          if (i + d < 8) ac[i + d] = mfma16(fa[it & 1][i], fb[it & 1][d], ac[i + d]);
          if (i < d && i + d < 8) ac[i + d] = mfma16(fa[it & 1][d], fb[it & 1][i], ac[i + d]);
        }
#pragma unroll
      for (int j = 0; j < 16; ++j) { SGB(0x8, 2); SGB(0x100, 1); }
      SGB(0x8, 4);
    }
    SB0;
    if (half) {
#pragma unroll
      for (int j = 0; j < 4; ++j) { const f32x4 t = ac[j]; ac[j] = ac[4 + j]; ac[4 + j] = t; asm volatile("" : "+v"(ac[j]), "+v"(ac[4 + j])); }
    }
#pragma unroll
    for (int j = 0; j < 4; ++j) sts128(lb + L2_PX + cw * 4096 + j * 1024 + lane * 16, __builtin_bit_cast(u32x4, ac[4 + j]));
    pair_sync(lb + L2_FL + cl * 4, 6u, lane);
    ST(1, 2);
    __builtin_amdgcn_s_setprio(3);
    const __amdgpu_buffer_rsrc_t ars = __builtin_amdgcn_make_buffer_rsrc(p.ws + OFF_AT, (short)0, SL * SD * 2, 0x00020000);
#pragma unroll
    for (int j = 0; j < 4; ++j) {
      const int l = 256 * (4 * half + j) + 16 * m + 4 * q;
      const u32x2 uq = *(const LAS u32x2*)(uintptr_t)(ui + 512 + l * 2);
      const f32x4 v = __builtin_bit_cast(f32x4, lds128(lb + L2_PX + (cw ^ 1) * 4096 + j * 1024 + lane * 16)) + ac[j];
      u32x2 o;
#pragma unroll
      for (int e = 0; e < 2; ++e) {
        const unsigned y = pk2(v[2 * e], v[2 * e + 1]), sk = pk2(bf2f(uq[e]) * dsk, bfhi(uq[e]) * dsk);
        const unsigned w = pk2(bf2f(y) + bf2f(sk), bfhi(y) + bfhi(sk));
        o[e] = w == ~0u ? 0x7fc07fc0u : w;
      }
      __builtin_amdgcn_raw_buffer_store_b64(o, ars, (ch * SL + l) * 2, 0, 16);
    }
    ST(1, 3);
    u32x4 av[2];
#pragma unroll 1
    for (int spin = 0; spin < (1 << 20); ++spin) {
      bool bad = false;
#pragma unroll
      for (int i = 0; i < 2; ++i) {
        av[i] = __builtin_amdgcn_raw_buffer_load_b128(ars, ((nt * 64 + 16 * cw + 8 * i + (lane >> 3)) * SL + mt * 64 + (lane & 7) * 8) * 2, 0, 16);
        bad |= av[i].x == ~0u || av[i].y == ~0u || av[i].z == ~0u || av[i].w == ~0u;
      }
      if (!__any(bad)) break;
      __builtin_amdgcn_s_sleep(8);
      asm volatile("" ::: "memory");
    }
#pragma unroll
    for (int i = 0; i < 2; ++i) sts128(lb + L2_PX + (cw ^ 1) * 4096 + (8 * i + (lane >> 3)) * ATS + (lane & 7) * 16, av[i]);
    ST(1, 5);
    __builtin_amdgcn_s_setprio(0);
  }
  bar();
  ST(1, 14);
  {
    const unsigned aa = lb + L2_PX + ((fo >> 1) ^ 1) * 4096 + (8 * (fo & 1) + ((lane & 15) >> 2)) * ATS + (16 * (wv & 3) + (lane & 3) * 4) * 2;
    const u32x4 av = __builtin_bit_cast(u32x4, trfrag(aa, aa + 4 * ATS));
    f32x4 s0 = {0.f, 0.f, 0.f, 0.f}, s1 = s0;
#pragma unroll
    for (int w = 0; w < 4; ++w) {
      s0 += __builtin_bit_cast(f32x4, lds128(lb + L2_RD + (w * 64 + ftok) * RS + fo * 32));
      s1 += __builtin_bit_cast(f32x4, lds128(lb + L2_RD + (w * 64 + ftok) * RS + fo * 32 + 16));
    }
    const float og = rbf(1.f - gate);
    u32x4 o;
#pragma unroll
    for (int e = 0; e < 4; ++e) {
      const float b0 = e < 2 ? s0[2 * e] : s1[2 * e - 4], b1 = e < 2 ? s0[2 * e + 1] : s1[2 * e - 3];
      o[e] = pk2(rbf(gate * bf2f(av[e])) + rbf(og * rbf(b0)), rbf(gate * bfhi(av[e])) + rbf(og * rbf(b1)));
    }
    *(u32x4*)(p.out + (size_t)(mt * 64 + ftok) * SD + nt * 64 + fo * 8) = o;
  }
  ST(1, 15);
}

extern "C" void kernel_launch(void* const* d_in, const int* in_sizes, int n_in, void* d_out, int out_size, void* d_ws,
                              size_t ws_size, hipStream_t stream) {
  P1 a;
  a.x = (const u16*)d_in[0]; a.w1 = (const u16*)d_in[10]; a.w3 = (const u16*)d_in[11]; a.w2 = (const u16*)d_in[12];
  a.gw = (const u16*)d_in[13]; a.ws = (char*)d_ws;
  P2 c;
  c.are = (const u16*)d_in[1]; c.aim = (const u16*)d_in[2]; c.bre = (const u16*)d_in[3]; c.bim = (const u16*)d_in[4];
  c.cre = (const u16*)d_in[5]; c.cim = (const u16*)d_in[6]; c.ldt = (const u16*)d_in[7]; c.dskip = (const u16*)d_in[8];
  c.out = (u16*)d_out; c.ws = (char*)d_ws;
  (void)hipFuncSetAttribute((const void*)k1, hipFuncAttributeMaxDynamicSharedMemorySize, L1_TOTAL);
  (void)hipFuncSetAttribute((const void*)k2, hipFuncAttributeMaxDynamicSharedMemorySize, L2_TOTAL);
  k1<<<256, 512, L1_TOTAL, stream>>>(a);
  k2<<<256, 512, L2_TOTAL, stream>>>(c);
}
```
